```python
import torch

def _softmax(x, axis=-1):
    return torch.softmax(x, dim=axis)
def _gelu(x):
    return torch.nn.functional.gelu(x, approximate="tanh")
def _silu(x):
    return torch.nn.functional.silu(x)
def _sigmoid(x):
    return torch.sigmoid(x)
def _layernorm(x, axis=-1, eps=1e-5):
    m = x.mean(dim=axis, keepdim=True)
    v = x.var(dim=axis, keepdim=True, unbiased=False)
    return (x - m) / torch.sqrt(v + eps)
def _scatter_add(tgt, idx, src, axis=0):
    return tgt.clone().index_add_(axis, idx, src)
def _segment_sum(src, seg, n):
    out = torch.zeros((n,) + src.shape[1:], dtype=src.dtype)
    return out.index_add_(0, seg, src)
def _pad(x, pw):
    flat = tuple(v for pair in reversed(pw) for v in pair)
    return torch.nn.functional.pad(x, flat)

kernel_name = 'gated_mix__linear_kernel_hedgehog__geglu_ffn__prompt'

def setup_inputs():

    _w = (lambda a: (lambda t: t.to(torch.bfloat16) if t.is_floating_point() else t)(torch.as_tensor(a)))
    def _A_setup():
        B, S, D, H = 1, 2048, 640, 32
        Dh = D // H
        return {
            "x": _w((lambda *s: torch.randn(*s, dtype=torch.bfloat16) * 0.05)(B, S, D)),
            "wqkv": _w((lambda *s: torch.randn(*s, dtype=torch.bfloat16) * 0.8)(D, 3 * D)),
            "w_phi": _w((lambda *s: torch.randn(*s, dtype=torch.bfloat16) * 0.8)(H, Dh, Dh) * (Dh ** -0.5)),
            "wo": _w((lambda *s: torch.randn(*s, dtype=torch.bfloat16) * 0.05)(D, D)),
        }
    _A_in = _A_setup()

    def _B_setup():
        B, S, D, F = 1, 2048, 640, 1728
        return {
            "x": _w((lambda *s: torch.randn(*s, dtype=torch.bfloat16) * 0.05)(B, S, D)),
            "w_gate": _w((lambda *s: torch.randn(*s, dtype=torch.bfloat16) * 0.05)(D, F)),
            "w_up": _w((lambda *s: torch.randn(*s, dtype=torch.bfloat16) * 0.05)(D, F)),
            "w_down": _w((lambda *s: torch.randn(*s, dtype=torch.bfloat16) * 0.05)(F, D)),
        }
    _B_in = _B_setup()

    return {
        "A_x": _A_in["x"],
        "A_wqkv": _A_in["wqkv"],
        "A_w_phi": _A_in["w_phi"],
        "A_wo": _A_in["wo"],
        "B_x": _B_in["x"],
        "B_w_gate": _B_in["w_gate"],
        "B_w_up": _B_in["w_up"],
        "B_w_down": _B_in["w_down"],
        "gate_w": _w((lambda *s: torch.randn(*s, dtype=torch.bfloat16) * 0.05)(640) * 16.0),
    }

def reference(A_x, A_wqkv, A_w_phi, A_wo, B_x, B_w_gate, B_w_up, B_w_down, gate_w):
    def _A_ref(x, wqkv, w_phi, wo):
        B, S, D = x.shape
        H = 32; Dh = D // H
        qkv = (lambda x, *d: x.permute(*d))((x @ wqkv).reshape(B, S, 3, H, Dh), 2, 0, 3, 1, 4)
        q, k, v = qkv[0], qkv[1], qkv[2]
        pq = torch.einsum("bhsd,hdf->bhsf", q, w_phi)
        pk = torch.einsum("bhsd,hdf->bhsf", k, w_phi)
        phi_q = torch.concatenate([_softmax(pq, axis=-1), _softmax(-pq, axis=-1)], axis=-1)
        phi_k = torch.concatenate([_softmax(pk, axis=-1), _softmax(-pk, axis=-1)], axis=-1)
        kv = torch.einsum("bhsf,bhsd->bhfd", phi_k, v)
        z  = torch.einsum("bhsf->bhf", phi_k)
        num = torch.einsum("bhqf,bhfd->bhqd", phi_q, kv)
        den = torch.einsum("bhqf,bhf->bhq", phi_q, z)[..., None] + 1e-6
        o = num / den
        return (lambda x, *d: x.permute(*d))(o, 0, 2, 1, 3).reshape(B, S, D) @ wo

    def _B_ref(x, w_gate, w_up, w_down):
        return (_gelu(x @ w_gate) * (x @ w_up)) @ w_down

    x = A_x
    g = _sigmoid((x * gate_w).sum(-1, keepdims=True))
    a = _A_ref(x, A_wqkv, A_w_phi, A_wo)
    b = _B_ref(x, B_w_gate, B_w_up, B_w_down)
    return g * a + (1 - g) * b
```

```cpp
#include <hip/hip_runtime.h>
#include <hip/hip_bf16.h>
typedef __hip_bfloat16 bf16;
constexpr int S=2048, D=640, H=32, DH=20, F=1728;

__device__ __forceinline__ float b2f(bf16 v){ return __bfloat162float(v); }

__global__ void gemm_naive(const bf16* A, const bf16* B, float* C, int M, int N, int K){
  __shared__ float As[16][17], Bs[16][17];
  int row = blockIdx.y*16+threadIdx.y, col = blockIdx.x*16+threadIdx.x;
  float acc=0;
  for(int k0=0;k0<K;k0+=16){
    As[threadIdx.y][threadIdx.x] = b2f(A[(size_t)row*K+k0+threadIdx.x]);
    Bs[threadIdx.y][threadIdx.x] = b2f(B[(size_t)(k0+threadIdx.y)*N+col]);
    __syncthreads();
    for(int k=0;k<16;k++) acc += As[threadIdx.y][k]*Bs[k][threadIdx.x];
    __syncthreads();
  }
  C[(size_t)row*N+col]=acc;
}
__global__ void gemm_naive_f(const float* A, const bf16* B, float* C, int M, int N, int K){
  __shared__ float As[16][17], Bs[16][17];
  int row = blockIdx.y*16+threadIdx.y, col = blockIdx.x*16+threadIdx.x;
  float acc=0;
  for(int k0=0;k0<K;k0+=16){
    As[threadIdx.y][threadIdx.x] = A[(size_t)row*K+k0+threadIdx.x];
    Bs[threadIdx.y][threadIdx.x] = b2f(B[(size_t)(k0+threadIdx.y)*N+col]);
    __syncthreads();
    for(int k=0;k<16;k++) acc += As[threadIdx.y][k]*Bs[k][threadIdx.x];
    __syncthreads();
  }
  C[(size_t)row*N+col]=acc;
}
__global__ void phi_kernel(const float* qkv, const bf16* wphi, float* phiq, float* phik){
  int idx = blockIdx.x*blockDim.x+threadIdx.x;
  if(idx>=S*H*2) return;
  int which = idx&1, h=(idx>>1)%H, s=(idx>>1)/H;
  const float* src = qkv + (size_t)s*3*D + which*D + h*DH;
  float p[DH];
  for(int f=0;f<DH;f++){ float a=0; for(int d=0;d<DH;d++) a += src[d]*b2f(wphi[(h*DH+d)*DH+f]); p[f]=a; }
  float mx=-1e30f, mn=1e30f;
  for(int f=0;f<DH;f++){ mx=fmaxf(mx,p[f]); mn=fminf(mn,p[f]); }
  float s1=0,s2=0;
  for(int f=0;f<DH;f++){ s1+=expf(p[f]-mx); s2+=expf(mn-p[f]); }
  float* dst = (which? phik:phiq) + ((size_t)s*H+h)*40;
  for(int f=0;f<DH;f++){ dst[f]=expf(p[f]-mx)/s1; dst[20+f]=expf(mn-p[f])/s2; }
}
__global__ void kv_kernel(const float* qkv, const float* phik, float* kv){
  int h=blockIdx.x; int t=threadIdx.x;
  if(t>=840) return;
  int f=t/21, d=t%21;
  float a=0;
  for(int s=0;s<S;s++){
    float v = d<20? qkv[(size_t)s*3*D+2*D+h*DH+d] : 1.0f;
    a += phik[((size_t)s*H+h)*40+f]*v;
  }
  kv[h*840+t]=a;
}
__global__ void o_kernel(const float* phiq, const float* kv, float* o){
  int idx = blockIdx.x*blockDim.x+threadIdx.x;
  if(idx>=S*H) return;
  int h=idx%H, s=idx/H;
  const float* p = phiq+(size_t)idx*40;
  float den=1e-6f; float den0=0;
  for(int f=0;f<40;f++) den0+=p[f]*kv[h*840+f*21+20];
  den+=den0;
  for(int d=0;d<20;d++){ float a=0; for(int f=0;f<40;f++) a+=p[f]*kv[h*840+f*21+d]; o[(size_t)s*D+h*DH+d]=a/den; }
}
__global__ void geglu_kernel(const float* g, const float* u, float* h){
  int idx = blockIdx.x*blockDim.x+threadIdx.x;
  if(idx>=S*F) return;
  float x=g[idx];
  float t = tanhf(0.7978845608028654f*(x+0.044715f*x*x*x));
  h[idx]=0.5f*x*(1+t)*u[idx];
}
__global__ void final_kernel(const bf16* x, const bf16* gw, const float* a, const float* b, bf16* out){
  int s=blockIdx.x; __shared__ float red[64];
  float acc=0; for(int d=threadIdx.x; d<D; d+=64) acc+=b2f(x[s*D+d])*b2f(gw[d]);
  for(int o=32;o>0;o>>=1) acc+=__shfl_xor(acc,o);
  float g=1.0f/(1.0f+expf(-acc));
  for(int d=threadIdx.x; d<D; d+=64) out[s*D+d]=__float2bfloat16(g*a[s*D+d]+(1-g)*b[s*D+d]);
}

extern "C" void kernel_launch(void* const* d_in, const int* in_sizes, int n_in,
                              void* d_out, int out_size, void* d_ws, size_t ws_size,
                              hipStream_t stream) {
  const bf16* x=(const bf16*)d_in[0]; const bf16* wqkv=(const bf16*)d_in[1]; const bf16* wphi=(const bf16*)d_in[2];
  const bf16* wo=(const bf16*)d_in[3]; const bf16* wg=(const bf16*)d_in[5]; const bf16* wu=(const bf16*)d_in[6];
  const bf16* wd=(const bf16*)d_in[7]; const bf16* gw=(const bf16*)d_in[8];
  float* ws=(float*)d_ws;
  float* qkv=ws; ws+=S*3*D;
  float* phiq=ws; ws+=S*H*40;
  float* phik=ws; ws+=S*H*40;
  float* kv=ws; ws+=H*840;
  float* o=ws; ws+=S*D;
  float* ga=ws; ws+=S*F;
  float* up=ws; ws+=S*F;
  float* hh=ws; ws+=S*F;
  float* a=ws; ws+=S*D;
  float* b=ws; ws+=S*D;
  dim3 blk(16,16);
  gemm_naive<<<dim3(3*D/16,S/16),blk,0,stream>>>(x,wqkv,qkv,S,3*D,D);
  gemm_naive<<<dim3(F/16,S/16),blk,0,stream>>>(x,wg,ga,S,F,D);
  gemm_naive<<<dim3(F/16,S/16),blk,0,stream>>>(x,wu,up,S,F,D);
  phi_kernel<<<(S*H*2+255)/256,256,0,stream>>>(qkv,wphi,phiq,phik);
  kv_kernel<<<H,896,0,stream>>>(qkv,phik,kv);
  o_kernel<<<(S*H+255)/256,256,0,stream>>>(phiq,kv,o);
  geglu_kernel<<<(S*F+255)/256,256,0,stream>>>(ga,up,hh);
  gemm_naive_f<<<dim3(D/16,S/16),blk,0,stream>>>(o,wo,a,S,D,D);
  gemm_naive_f<<<dim3(D/16,S/16),blk,0,stream>>>(hh,wd,b,S,D,F);
  final_kernel<<<S,64,0,stream>>>(x,gw,a,b,(bf16*)d_out);
}
```

```cpp
#include <hip/hip_runtime.h>

typedef unsigned short u16;
typedef unsigned v4u __attribute__((ext_vector_type(4)));
typedef unsigned v2u __attribute__((ext_vector_type(2)));
typedef float f32x4 __attribute__((ext_vector_type(4)));
typedef float f32x2 __attribute__((ext_vector_type(2)));
typedef short s16x4 __attribute__((ext_vector_type(4)));
typedef __bf16 bf16x8 __attribute__((ext_vector_type(8)));
typedef __bf16 bf16x2 __attribute__((ext_vector_type(2)));
#define LAS __attribute__((address_space(3)))
#define GAS __attribute__((address_space(1)))
#define DI __device__ __forceinline__

constexpr int S_ = 2048, D_ = 640, F_ = 1728;
constexpr int NKS = 74, NKH = 54;
constexpr unsigned ACT_OFF = 0, ACT_BYTES = 32u * NKS * 4 * 1024;
constexpr unsigned WT_OFF = ACT_OFF + ACT_BYTES, WT_BYTES = 8u * NKS * 5 * 1024;
constexpr unsigned Q_OFF = WT_OFF + WT_BYTES, Q_BYTES = 32u * 128 * 1024;
constexpr unsigned KVP_REC = 3360;
constexpr unsigned KVP_OFF = Q_OFF + Q_BYTES, KVP_BYTES = 32u * 16 * KVP_REC;
constexpr unsigned G_OFF = KVP_OFF + KVP_BYTES, G_BYTES = S_ * 4;
constexpr unsigned WPF_OFF = G_OFF + G_BYTES, WPF_BYTES = 32u * 2 * 1024;
constexpr unsigned WS_BYTES = WPF_OFF + WPF_BYTES;
constexpr unsigned SENT = 0xFFFFFFFFu;

constexpr unsigned L_W = 0, WSLOT = 24576, L_X = 4 * WSLOT, XSLOT = 16384, L_WPHI = L_X + 2 * XSLOT, L_ZERO = L_WPHI + 6400,
                   K1_LDS = L_WPHI + 8192;
constexpr unsigned L_PK = 0, L_V = 8 * 128 * 80;

DI unsigned rfl(unsigned x) { return __builtin_amdgcn_readfirstlane(x); }
DI unsigned cvtpk(f32x2 v) {
  bf16x2 b = __builtin_convertvector(v, bf16x2);
  return __builtin_bit_cast(unsigned, b);
}
DI unsigned cvtpk(float lo, float hi) { return cvtpk(f32x2{lo, hi}); }
DI v2u cvt4(f32x4 v) { return v2u{cvtpk(v[0], v[1]), cvtpk(v[2], v[3])}; }
DI v4u cvt8(f32x4 a, f32x4 b) { return v4u{cvtpk(a[0], a[1]), cvtpk(a[2], a[3]), cvtpk(b[0], b[1]), cvtpk(b[2], b[3])}; }
DI float bf2f(unsigned b) { return __builtin_bit_cast(float, b << 16); }
DI f32x4 mfma(v4u a, v4u b, f32x4 c) {
  return __builtin_amdgcn_mfma_f32_16x16x32_bf16(__builtin_bit_cast(bf16x8, a), __builtin_bit_cast(bf16x8, b), c, 0, 0, 0);
}
DI v2u tr_read(LAS char* p) {
  s16x4 r = __builtin_amdgcn_ds_read_tr16_b64_v4i16((LAS s16x4*)p);
  return __builtin_bit_cast(v2u, r);
}
DI v4u tr_read2(LAS char* p, int d) {
  v2u a = tr_read(p), b = tr_read(p + d);
  return v4u{a.x, a.y, b.x, b.y};
}
DI __amdgpu_buffer_rsrc_t mk_rsrc(const void* p, unsigned bytes) {
  return __builtin_amdgcn_make_buffer_rsrc((void*)p, (short)0, (int)bytes, 0x00020000);
}
DI float u2f(unsigned u) { return __builtin_bit_cast(float, u); }
DI unsigned f2u(float f) { return __builtin_bit_cast(unsigned, f); }
DI float red4_max(float x) {
  auto r = __builtin_amdgcn_permlane16_swap(f2u(x), f2u(x), false, false);
  unsigned r0 = r[0], r1 = r[1];
  x = fmaxf(u2f(r0), u2f(r1));
  auto t = __builtin_amdgcn_permlane32_swap(f2u(x), f2u(x), false, false);
  r0 = t[0]; r1 = t[1];
  return fmaxf(u2f(r0), u2f(r1));
}
DI float red4_sum(float x) {
  auto r = __builtin_amdgcn_permlane16_swap(f2u(x), f2u(x), false, false);
  unsigned r0 = r[0], r1 = r[1];
  x = u2f(r0) + u2f(r1);
  auto t = __builtin_amdgcn_permlane32_swap(f2u(x), f2u(x), false, false);
  r0 = t[0]; r1 = t[1];
  return u2f(r0) + u2f(r1);
}
DI f32x2 exp2v(f32x2 a) { return f32x2{__builtin_amdgcn_exp2f(a[0]), __builtin_amdgcn_exp2f(a[1])}; }
DI void st16(__amdgpu_buffer_rsrc_t r, unsigned off, v4u v) { __builtin_amdgcn_raw_buffer_store_b128(v, r, off, 0, 16); }
DI void st8(__amdgpu_buffer_rsrc_t r, unsigned off, v2u v) { __builtin_amdgcn_raw_buffer_store_b64(v, r, off, 0, 16); }
#define SB0() __builtin_amdgcn_sched_barrier(0)
#define SYNC(n) do { SB0(); asm volatile("s_waitcnt vmcnt(" #n ") lgkmcnt(0)\n\ts_barrier" ::: "memory"); SB0(); } while (0)
#define LDSDMA(m0v, vo, rs, so) \
  asm volatile("s_mov_b32 m0, %0\n\ts_nop 0\n\tbuffer_load_dwordx4 %1, %2, %3 offen lds" :: "s"(m0v), "v"(vo), "s"(rs), "s"(so) : "memory")

#define STAMP_DECL
#define STAMP_AT(kern, i)
#define STAMP_FLUSH(kern)
#define STAMP_ARG
#define STAMP_PASS

struct K1Args {
  const u16 *x, *wqkv, *wphi, *wo, *wg, *wu, *wd, *gw;
  char* ws;
};
struct K2Args {
  char* ws;
  u16* out;
};

#define SYNCL() do { SB0(); asm volatile("s_waitcnt lgkmcnt(0)\n\ts_barrier" ::: "memory"); SB0(); } while (0)
#define DMA(m0v, vo, rs, so, off) \
  asm volatile("s_mov_b32 m0, %0\n\ts_nop 0\n\tbuffer_load_dwordx4 %1, %2, %3 offen offset:%4 lds" :: "s"(m0v), "v"(vo), "s"(rs), "s"(so), "i"(off) : "memory")

template <bool GU, int B> DI LAS char* a_addr(LAS char* wbase, const unsigned (&aoff)[5]) {
  return wbase + aoff[GU ? B >> 1 : B] + ((GU && (B & 1)) ? 12288 : 0);
}
DI void mfma_ip(f32x4& c, const v4u& a, const v4u& b) {
  asm volatile("v_mfma_f32_16x16x32_bf16 %0, %1, %2, %0" : "+v"(c) : "v"(a), "v"(b));
}
template <bool GU, bool NEXT, int T, int J = 0>
DI void k1_step(f32x4 (&acc)[4][GU ? 6 : 5], v4u (&a)[4], v4u (&x)[2][4], LAS char* L, const unsigned (&aoff)[5],
                const unsigned (&xoff)[2]) {
  constexpr int NB = GU ? 6 : 5, R0 = T * NB;
  if constexpr (J < 4 * NB) {
    constexpr int b = J >> 2, r = J & 3, nb = b + 2;
    if constexpr (r < 2) {
      if constexpr (nb < NB || NEXT) {
        v2u t = tr_read(a_addr<GU, nb % NB>(L + L_W + ((nb < NB ? T : T + 1) & 3) * WSLOT, aoff) + r * 1536);
        if constexpr (r == 0) { a[(R0 + nb) & 3].x = t.x; a[(R0 + nb) & 3].y = t.y; }
        else { a[(R0 + nb) & 3].z = t.x; a[(R0 + nb) & 3].w = t.y; }
      }
    } else if constexpr (r == 2 && b < 4 && NEXT)
      x[(T + 1) & 1][b] = *(LAS v4u*)(L + L_X + (((T + 1) >> 1) & 1) * XSLOT + xoff[(T + 1) & 1] + b * 2048);
    mfma_ip(acc[r][b], a[(R0 + b) & 3], x[T & 1][r]);
    SB0();
    k1_step<GU, NEXT, T, J + 1>(acc, a, x, L, aoff, xoff);
  }
}
template <bool GU>
DI void k1_mainloop(const K1Args& p, f32x4 (&acc)[4][GU ? 6 : 5], LAS char* L, const unsigned (&aoff)[5], unsigned xoff0 STAMP_ARG) {
  const unsigned xoff[2] = {xoff0, xoff0 ^ 64};
  v4u a[4], x[2][4];
  SYNCL();
  STAMP_AT(0, 4);
#pragma unroll
  for (int m = 0; m < 4; ++m) x[0][m] = *(LAS v4u*)(L + L_X + xoff0 + m * 2048);
  a[0] = tr_read2(a_addr<GU, 0>(L + L_W, aoff), 1536);
  a[1] = tr_read2(a_addr<GU, 1>(L + L_W, aoff), 1536);
#pragma unroll 1
  for (int i = 0; i < 16; i += 4) {
    SYNCL(); k1_step<GU, true, 0>(acc, a, x, L, aoff, xoff);
    SYNCL(); k1_step<GU, true, 1>(acc, a, x, L, aoff, xoff);
    SYNCL(); k1_step<GU, true, 2>(acc, a, x, L, aoff, xoff);
    SYNCL(); k1_step<GU, true, 3>(acc, a, x, L, aoff, xoff);
  }
  SYNCL(); k1_step<GU, true, 0>(acc, a, x, L, aoff, xoff);
  SYNCL(); k1_step<GU, true, 1>(acc, a, x, L, aoff, xoff);
  SYNCL(); k1_step<GU, true, 2>(acc, a, x, L, aoff, xoff);
  k1_step<GU, false, 3>(acc, a, x, L, aoff, xoff);
  asm volatile("s_nop 15" ::: "memory");
  SB0();
}

DI unsigned gelu_mul2(f32x2 x, f32x2 u) {
  const float k0 = -2.0f * 0.7978845608028654f * 1.4426950408889634f, k1 = k0 * 0.044715f;
  f32x2 t = x * x;
  f32x2 a = x * (t * k1 + k0);
  f32x2 d = exp2v(a) + 1.0f;
  f32x2 r = f32x2{__builtin_amdgcn_rcpf(d[0]), __builtin_amdgcn_rcpf(d[1])};
  return cvtpk((x * u) * r);
}
DI v2u gelu_mul4(f32x4 x, f32x4 u) {
  return v2u{gelu_mul2(f32x2{x[0], x[1]}, f32x2{u[0], u[1]}), gelu_mul2(f32x2{x[2], x[3]}, f32x2{u[2], u[3]})};
}

__global__ __launch_bounds__(768, 3) void k1(K1Args p) {
  extern __shared__ __attribute__((aligned(16))) char smem[];
  LAS char* L = (LAS char*)smem;
  const int tid = threadIdx.x, lane = tid & 63, w = rfl(tid >> 6);
  const int bid = blockIdx.x, xcd = bid & 7, idx = bid >> 3;
  const unsigned long long CSMAP = 0xDC87BA65E943F210ull;
  const int rt = (xcd & 1) * 8 + (idx & 7), cs = (int)(CSMAP >> (4 * ((xcd >> 1) * 4 + (idx >> 3)))) & 15;
  const int g = lane >> 4, q = (lane >> 2) & 3, pp = lane & 3, tok = lane & 15;
  char* ws = p.ws;
  const __amdgpu_buffer_rsrc_t rws = mk_rsrc(ws, WS_BYTES);
  STAMP_DECL;
  STAMP_AT(0, 0);

  if (cs == 15) {
    if (w >= 8) return;
    const __amdgpu_buffer_rsrc_t rd = mk_rsrc(p.wd, F_ * D_ * 2), ro = mk_rsrc(p.wo, D_ * D_ * 2);
    unsigned rowi[5], chb[5];
#pragma unroll
    for (int j = 0; j < 5; ++j) {
      const int Lc = (w * 5 + j) * 64 + lane;
      rowi[j] = Lc / 80; chb[j] = (Lc % 80) * 16;
    }
#pragma unroll 1
    for (int round = 0; round < 2; ++round) {
      const int nsl = round == 0 ? 3 : (rt + 64 < NKS ? 2 : 1);
#pragma unroll 1
      for (int sl = 0; sl < nsl; ++sl) {
        const int s = rt + 16 * (round * 3 + sl);
        int b0, b1;
        if (s < NKH) {
          int u = s / 6, e = s % 6;
          if (e < 4) { b0 = 192 * u + 48 * e; b1 = b0 + 16; } else { b0 = 192 * u + 96 * (e - 4) + 32; b1 = b0 + 48; }
        } else { b0 = 32 * (s - NKH); b1 = b0 + 16; }
        const __amdgpu_buffer_rsrc_t rs = s < NKH ? rd : ro;
#pragma unroll
        for (int j = 0; j < 5; ++j) {
          const unsigned vo = (rowi[j] < 16 ? b0 + rowi[j] : b1 + rowi[j] - 16) * 1280 + chb[j];
          const unsigned m0 = (unsigned)(sl * 40960 + (w * 5 + j) * 1024);
          LDSDMA(m0, vo, rs, 0);
        }
      }
      if (round == 0) {
        v4u gwv[5], xv[4][5];
#pragma unroll
        for (int i = 0; i < 5; ++i) gwv[i] = *(const v4u*)(p.gw + (tok + 16 * i) * 8);
#pragma unroll
        for (int it = 0; it < 4; ++it)
#pragma unroll
          for (int i = 0; i < 5; ++i) xv[it][i] = *(const v4u*)(p.x + (rt * 128 + w * 16 + it * 4 + g) * D_ + (tok + 16 * i) * 8);
        const int head = 2 * rt + ((w >> 1) & 1), f = 16 * (w & 1) + tok;
        const u16* wp = p.wphi + head * 400 + (f < 20 ? f : 0);
        unsigned e[5];
#pragma unroll
        for (int j = 0; j < 4; ++j) e[j] = wp[(4 * g + j) * 20];
        e[4] = wp[(16 + g) * 20];
        float a[4];
#pragma unroll
        for (int it = 0; it < 4; ++it) {
          float s0 = 0.f, s1 = 0.f;
#pragma unroll
          for (int i = 0; i < 5; ++i)
#pragma unroll
            for (int k = 0; k < 4; ++k) {
              const unsigned xe = xv[it][i][k], ge = gwv[i][k];
              s0 = __builtin_fmaf(bf2f(xe & 0xffffu), bf2f(ge & 0xffffu), s0);
              s1 = __builtin_fmaf(u2f(xe & 0xffff0000u), u2f(ge & 0xffff0000u), s1);
            }
          float s = s0 + s1;
          s += __shfl_xor(s, 1); s += __shfl_xor(s, 2); s += __shfl_xor(s, 4); s += __shfl_xor(s, 8);
          a[it] = s;
        }
        const float av = tok == 0 ? a[0] : tok == 1 ? a[1] : tok == 2 ? a[2] : a[3];
        if (tok < 4)
          __builtin_amdgcn_raw_buffer_store_b32(f2u(1.0f / (1.0f + __expf(-av))), rws, G_OFF + (rt * 128 + w * 16 + tok * 4 + g) * 4, 0, 16);
        v4u fr = v4u{e[0] | e[1] << 16, e[2] | e[3] << 16, e[4], 0u};
        if (f >= 20) fr = v4u{0u, 0u, 0u, 0u};
        if (w < 4) st16(rws, WPF_OFF + (head * 2 + (w & 1)) * 1024 + lane * 16, fr);
      }
      SYNC(0);
#pragma unroll 1
      for (int sl = 0; sl < nsl; ++sl) {
        const int s = rt + 16 * (round * 3 + sl);
        LAS char* base = L + sl * 40960 + (4 * g + q) * 1280 + w * 160;
#pragma unroll
        for (int nb = 0; nb < 5; ++nb) {
          const int col = nb < 4 ? 32 * (nb >> 1) + 8 * pp + 4 * (nb & 1) : 64 + 4 * pp;
          v4u fr = tr_read2(base + col * 2, 16 * 1280);
          st16(rws, WT_OFF + ((w * NKS + s) * 5 + nb) * 1024 + lane * 16, fr);
        }
      }
      asm volatile("s_waitcnt lgkmcnt(0)\n\ts_barrier" ::: "memory");
      if (round == 0) STAMP_AT(0, 1); else STAMP_AT(0, 2);
    }
    STAMP_FLUSH(0);
    return;
  }

  const bool isGU = cs < 9, isKV = cs >= 11;
  if (w >= 8) {
    const int lw = w - 8, sub = lw >> 1;
    const __amdgpu_buffer_rsrc_t rx = mk_rsrc(p.x + rt * 128 * D_, (unsigned)((S_ - rt * 128) * D_ * 2));
    const int r0 = 32 * lw + (lane >> 3);
    const unsigned vx0 = (unsigned)(r0 * 1280 + (((lane & 7) ^ ((r0 >> 1) & 7)) * 16)), vx1 = (vx0 ^ 64u) + 8 * 1280 - 1024;
    const unsigned m0x = L_X + lw * 4096;
#define LOAD_X(stage, slot, e) do { const unsigned m0_ = m0x + (unsigned)(slot) * XSLOT + (e) * 2048, so_ = (unsigned)(stage) * 128 + (e) * 16 * 1280; \
      DMA(m0_, vx0, rx, so_, 0); DMA(m0_, vx1, rx, so_, 1024); } while (0)
    LOAD_X(0, 0, 0); LOAD_X(0, 0, 1);
    const char* base;
    unsigned pitch, total, boff;
    if (isGU) {
      base = (const char*)(sub ? p.wu : p.wg); pitch = F_ * 2; boff = 384u * cs; total = D_ * F_ * 2;
    } else {
      base = (const char*)p.wqkv; pitch = 3 * D_ * 2; total = D_ * 3 * D_ * 2;
      if (!isKV) boff = 640u * (cs - 9) + (sub ? 256u : 0u);
      else { const int j = cs - 11; boff = 1280u + 320u * j - ((j & 1) ? 64u : 0u) + (sub ? 1280u : 0u); }
    }
    const __amdgpu_buffer_rsrc_t rw = mk_rsrc(base + boff + (lw & 1) * 16 * pitch, total - boff - (lw & 1) * 16 * pitch);
    unsigned vw[6];
#pragma unroll
    for (int j = 0; j < 3; ++j) {
      const int Lc = 64 * j + lane, kr = Lc / 24, pos = Lc % 24;
      const unsigned c0 = (unsigned)((pos >> 3) * 128 + (((pos & 7) ^ (kr & 2)) * 16));
      vw[j] = (unsigned)kr * pitch + c0 - 1024u * j;
      vw[3 + j] = (unsigned)(kr + 8) * pitch + (c0 ^ 64u) - 1024u * j;
    }
    const unsigned m0w = L_W + sub * 12288 + (lw & 1) * 6144, wstep = 32 * pitch;
#define LOAD_W(s) do { const unsigned m0_ = m0w + (unsigned)((s) & 3) * WSLOT, so_ = (unsigned)(s) * wstep; \
      DMA(m0_, vw[0], rw, so_, 0); DMA(m0_, vw[1], rw, so_, 1024); DMA(m0_, vw[2], rw, so_, 2048); \
      DMA(m0_ + 3072, vw[3], rw, so_, 0); DMA(m0_ + 3072, vw[4], rw, so_, 1024); DMA(m0_ + 3072, vw[5], rw, so_, 2048); } while (0)
    LOAD_W(0);
    LOAD_W(1); LOAD_X(1, 1, 0); LOAD_X(1, 1, 1);
    LOAD_W(2);
    SYNC(16);
    SYNC(6); LOAD_W(3);
#pragma unroll 1
    for (int i = 1; i < 15; i += 2) {
      SYNC(6); LOAD_X((i + 3) >> 1, ((i + 3) >> 1) & 1, 0); LOAD_X((i + 3) >> 1, ((i + 3) >> 1) & 1, 1); LOAD_W(i + 3);
      SYNC(10); LOAD_W(i + 4);
    }
    SYNC(6); LOAD_X(9, 1, 0); LOAD_X(9, 1, 1); LOAD_W(18);
    SYNC(10); LOAD_W(19);
    if (isKV) {
      const __amdgpu_buffer_rsrc_t rp = mk_rsrc(p.wphi + 8 * (cs - 11) * 400, 6400);
      DMA(L_WPHI + lw * 2048, (unsigned)(lw * 2048 + lane * 16), rp, 0, 0);
      DMA(L_WPHI + lw * 2048, (unsigned)(lw * 2048 + lane * 16), rp, 0, 1024);
    }
    SYNC(6);
    SYNC(0);
    return;
  }
  const int mh = w & 1, n = w >> 1;
  const int sw = ((q >> 1) & 1) | ((g & 1) << 1);
  const unsigned xoff0 = (unsigned)((64 * mh + tok) * 128 + ((g ^ (tok >> 1)) * 16));
  const int rt64 = rt * 2 + mh;

  if (isGU) {
    unsigned aoff[5];
#pragma unroll
    for (int b = 0; b < 3; ++b) {
      const int ch = 6 * n + 2 * b + (pp >> 1);
      aoff[b] = (unsigned)((8 * g + q) * 384 + (ch >> 3) * 128 + (((ch & 7) ^ (2 * sw)) * 16) + (pp & 1) * 8);
    }
    aoff[3] = aoff[4] = 0;
    f32x4 acc[4][6];
#pragma unroll
    for (int m = 0; m < 4; ++m)
#pragma unroll
      for (int b = 0; b < 6; ++b) acc[m][b] = f32x4{0.f, 0.f, 0.f, 0.f};
    k1_mainloop<true>(p, acc, L, aoff, xoff0 STAMP_PASS);
    STAMP_AT(0, 1);
    const unsigned actA = ACT_OFF + ((rt64 * NKS + 6 * cs + n) * 4) * 1024 + lane * 16;
    const unsigned actB = ACT_OFF + ((rt64 * NKS + 6 * cs + 4 + (n >> 1)) * 4 + 2 * (n & 1)) * 1024 + lane * 16;
    v2u h2[4];
#pragma unroll
    for (int m = 0; m < 4; ++m) h2[m] = gelu_mul4(acc[m][4], acc[m][5]);
    const bool odd = n & 1;
    *(LAS v4u*)(L + L_WPHI + w * 1024 + lane * 16) = odd ? v4u{h2[0].x, h2[0].y, h2[1].x, h2[1].y} : v4u{h2[2].x, h2[2].y, h2[3].x, h2[3].y};
    const v2u k0 = odd ? h2[2] : h2[0], k1 = odd ? h2[3] : h2[1];
#pragma unroll
    for (int m = 0; m < 4; ++m) {
      const v2u h0 = gelu_mul4(acc[m][0], acc[m][1]), h1 = gelu_mul4(acc[m][2], acc[m][3]);
      st16(rws, actA + m * 1024, v4u{h0.x, h0.y, h1.x, h1.y});
      if (m == 1) {
        SYNCL();
        const v4u o = *(LAS v4u*)(L + L_WPHI + (w ^ 2) * 1024 + lane * 16);
        st16(rws, actB, odd ? v4u{o.x, o.y, k0.x, k0.y} : v4u{k0.x, k0.y, o.x, o.y});
        st16(rws, actB + 1024, odd ? v4u{o.z, o.w, k1.x, k1.y} : v4u{k1.x, k1.y, o.z, o.w});
      }
    }
    STAMP_AT(0, 2);
    STAMP_FLUSH(0);
    return;
  }

  unsigned aoff[5];
  {
    const int offch = isKV ? ((cs - 11) & 1) * 4 : (n >> 1) * 4;
#pragma unroll
    for (int b = 0; b < 5; ++b) {
      const int u = 2 * offch + 20 * (n & 1) + 5 * pp + b, ch = u >> 1;
      aoff[b] = (unsigned)((n >> 1) * 12288 + (8 * g + q) * 384 + (ch >> 3) * 128 + (((ch & 7) ^ (2 * sw)) * 16) + (u & 1) * 8);
    }
  }
  f32x4 acc[4][5];
#pragma unroll
  for (int m = 0; m < 4; ++m)
#pragma unroll
    for (int b = 0; b < 5; ++b) acc[m][b] = f32x4{0.f, 0.f, 0.f, 0.f};
  k1_mainloop<false>(p, acc, L, aoff, xoff0 STAMP_PASS);
  STAMP_AT(0, 1);

  if (!isKV) {
    const int head = 16 * (cs - 9) + 4 * n + g;
    const unsigned qp = Q_OFF + (head * 128 + rt64 * 4) * 1024 + tok * 16;
#pragma unroll
    for (int m = 0; m < 4; ++m) {
      const v2u c4 = cvt4(acc[m][4]);
#pragma unroll
      for (int b = 0; b < 4; ++b) {
        const v2u c = cvt4(acc[m][b]);
        const unsigned e = b < 2 ? c4.x : c4.y;
        st16(rws, qp + m * 1024 + b * 256, v4u{c.x, c.y, (b & 1) ? e >> 16 : e & 0xffffu, 0u});
      }
    }
    const unsigned op = ACT_OFF + ((2 * rt + cs - 9) * NKS + NKH) * 4096 + tid * 16;
#pragma unroll
    for (int i = 0; i < 10; ++i) st16(rws, op + i * 8192, v4u{SENT, SENT, SENT, SENT});
    STAMP_AT(0, 2);
    STAMP_FLUSH(0);
    return;
  }

  const int hl = 4 * (n & 1) + g;
  v4u c01[4], c23[4];
  v2u c4[4];
#pragma unroll
  for (int m = 0; m < 4; ++m) { c01[m] = cvt8(acc[m][0], acc[m][1]); c23[m] = cvt8(acc[m][2], acc[m][3]); c4[m] = cvt4(acc[m][4]); }
  SYNCL();
  if (n < 2) {
    v4u wf[5][3];
    LAS char* wb = L + (pp == g ? L_WPHI + (unsigned)(hl * 800 + q * 40) : L_ZERO);
#pragma unroll
    for (int bo = 0; bo < 5; ++bo)
#pragma unroll
      for (int s3 = 0; s3 < 3; ++s3) {
        v2u lo = tr_read(wb + s3 * 320 + bo * 8), hi = v2u{0u, 0u};
        if (s3 < 2) hi = tr_read(wb + s3 * 320 + 160 + bo * 8);
        wf[bo][s3] = v4u{lo.x, lo.y, hi.x, hi.y};
      }
    LAS char* dst0 = L + L_PK + (hl * 128 + 64 * mh + tok) * 80;
#pragma unroll
    for (int m = 0; m < 4; ++m) {
      const v4u kf[3] = {c01[m], c23[m], v4u{c4[m].x, c4[m].y, 0u, 0u}};
      f32x4 pk[5];
#pragma unroll
      for (int bo = 0; bo < 5; ++bo) {
        pk[bo] = f32x4{0.f, 0.f, 0.f, 0.f};
#pragma unroll
        for (int s3 = 0; s3 < 3; ++s3) pk[bo] = mfma(wf[bo][s3], kf[s3], pk[bo]);
      }
      float mx = pk[0][0], mn = pk[0][0];
#pragma unroll
      for (int bo = 0; bo < 5; ++bo)
#pragma unroll
        for (int r = 0; r < 4; ++r) { mx = fmaxf(mx, pk[bo][r]); mn = fminf(mn, pk[bo][r]); }
      const float l2e = 1.4426950408889634f;
      const float mxs = -mx * l2e, mns = mn * l2e;
      f32x2 ep[10], en[10], sp = {0.f, 0.f}, sn = {0.f, 0.f};
#pragma unroll
      for (int i = 0; i < 10; ++i) {
        const f32x2 v = f32x2{pk[i >> 1][2 * (i & 1)], pk[i >> 1][2 * (i & 1) + 1]};
        ep[i] = exp2v(v * l2e + mxs);
        en[i] = exp2v(v * -l2e + mns);
        sp += ep[i]; sn += en[i];
      }
      const float rp = __builtin_amdgcn_rcpf(sp[0] + sp[1]), rn = __builtin_amdgcn_rcpf(sn[0] + sn[1]);
      unsigned o[20];
#pragma unroll
      for (int i = 0; i < 10; ++i) { o[i] = cvtpk(ep[i] * rp); o[10 + i] = cvtpk(en[i] * rn); }
      LAS char* dst = dst0 + m * 16 * 80;
#pragma unroll
      for (int i = 0; i < 5; ++i) *(LAS v4u*)(dst + i * 16) = v4u{o[4 * i], o[4 * i + 1], o[4 * i + 2], o[4 * i + 3]};
    }
  } else {
#pragma unroll
    for (int m = 0; m < 4; ++m) {
      LAS char* dst = L + L_V + (hl * 128 + 64 * mh + 16 * m + tok) * 48;
      *(LAS v4u*)(dst) = c01[m];
      *(LAS v4u*)(dst + 16) = c23[m];
      *(LAS v4u*)(dst + 32) = v4u{c4[m].x, c4[m].y, 0x00003f80u, 0u};
    }
  }
  STAMP_AT(0, 2);
  __syncthreads();
  {
    f32x4 kv[3][2];
#pragma unroll
    for (int fb = 0; fb < 3; ++fb) { kv[fb][0] = f32x4{0.f, 0.f, 0.f, 0.f}; kv[fb][1] = f32x4{0.f, 0.f, 0.f, 0.f}; }
    LAS char* pa = L + L_PK + (w * 128 + 8 * g + q) * 80 + pp * 8;
    LAS char* pb = L + L_V + (w * 128 + 8 * g + q) * 48 + pp * 8;
#pragma unroll
    for (int ks = 0; ks < 4; ++ks) {
      v4u af[3], bf[2];
#pragma unroll
      for (int fb = 0; fb < 3; ++fb) af[fb] = tr_read2(pa + ks * 32 * 80 + fb * 32, 4 * 80);
#pragma unroll
      for (int vb = 0; vb < 2; ++vb) bf[vb] = tr_read2(pb + ks * 32 * 48 + vb * 32, 4 * 48);
#pragma unroll
      for (int fb = 0; fb < 3; ++fb)
#pragma unroll
        for (int vb = 0; vb < 2; ++vb) kv[fb][vb] = mfma(af[fb], bf[vb], kv[fb][vb]);
    }
    const int head = 8 * (cs - 11) + w;
    const unsigned rec = KVP_OFF + (head * 16 + rt) * KVP_REC;
    const unsigned o4 = rec + tok * 64 + g * 16, o2 = g < 2 ? rec + tok * 32 + g * 16 : 0x80000000u;
    const unsigned t5 = tok < 5 ? 0u : 0x80000000u;
    st16(rws, o4, __builtin_bit_cast(v4u, kv[0][0]));
    st16(rws, o4 + 1024, __builtin_bit_cast(v4u, kv[1][0]));
    st16(rws, o2 + 2048, __builtin_bit_cast(v4u, kv[2][0]));
    st16(rws, (o4 | t5) + 2560, __builtin_bit_cast(v4u, kv[0][1]));
    st16(rws, (o4 | t5) + 2880, __builtin_bit_cast(v4u, kv[1][1]));
    st16(rws, (o2 | t5) + 3200, __builtin_bit_cast(v4u, kv[2][1]));
  }
  STAMP_AT(0, 3);
  STAMP_FLUSH(0);
}

constexpr unsigned L2_RED = 0, L2_REDO = 61440, L2_KVS = L2_REDO + 81920, L2_FLAG = L2_KVS + 21 * 80 + 16, K2_LDS = L2_FLAG + 64;
#define OLD_AUX 16
#define K2_P 2

struct Stage { v4u a[5], b[4]; };
DI void k2_load(Stage& st, __amdgpu_buffer_rsrc_t ra, __amdgpu_buffer_rsrc_t rw, const unsigned (&vb)[4], unsigned vo, int ks) {
#pragma unroll
  for (int i = 0; i < 4; ++i) st.b[i] = __builtin_amdgcn_raw_buffer_load_b128(ra, vb[i], ks * 4096, 0);
#pragma unroll
  for (int nb = 0; nb < 5; ++nb) st.a[nb] = __builtin_amdgcn_raw_buffer_load_b128(rw, vo + nb * 1024, ks * 5120, 0);
}

template <int NH>
DI void k2_gemm(f32x4 (&acc)[5][4], __amdgpu_buffer_rsrc_t ra, __amdgpu_buffer_rsrc_t rw, const unsigned (&vb)[4], unsigned vo, int h0) {
  Stage st[K2_P];
#pragma unroll
  for (int i = 0; i < K2_P; ++i) k2_load(st[i], ra, rw, vb, vo, h0 + i);
  SB0();
#pragma unroll
  for (int i = 0; i < NH; ++i) {
    Stage& cur = st[i % K2_P];
#pragma unroll
    for (int nb = 0; nb < 5; ++nb)
#pragma unroll
      for (int m = 0; m < 4; ++m) acc[nb][m] = mfma(cur.a[nb], cur.b[m], acc[nb][m]);
    if (i + K2_P < NH) k2_load(cur, ra, rw, vb, vo, h0 + i + K2_P);
    SB0();
  }
}

__global__ __launch_bounds__(512, 2) void k2(K2Args p) {
  extern __shared__ __attribute__((aligned(16))) char smem[];
  LAS char* L = (LAS char*)smem;
  const int tid = threadIdx.x, lane = tid & 63, w = rfl(tid >> 6);
  const int bid = blockIdx.x, xcd = bid & 7, idx = bid >> 3;
  const int g = lane >> 4, tok = lane & 15;
  char* ws = p.ws;
  STAMP_DECL;
  STAMP_AT(1, 0);
  const int rt64 = (xcd >> 1) * 8 + (idx & 7), ct = (xcd & 1) * 4 + (idx >> 3);
  const __amdgpu_buffer_rsrc_t ra = mk_rsrc(ws + ACT_OFF + rt64 * (NKS * 4096), NKS * 4096);
  const __amdgpu_buffer_rsrc_t rw = mk_rsrc(ws + WT_OFF + ct * (NKS * 5120), NKS * 5120);
  const f32x4 z4 = {0.f, 0.f, 0.f, 0.f};
  f32x4 acc[5][4];
#pragma unroll
  for (int nb = 0; nb < 5; ++nb)
#pragma unroll
    for (int m = 0; m < 4; ++m) acc[nb][m] = z4;

  if (w >= 4) {
#define EARLYBAR
#define OPRIO 2
    __builtin_amdgcn_s_setprio(OPRIO);
    if (tid == 256) *(volatile LAS unsigned*)(L + L2_FLAG) = 0u;
    asm volatile("s_waitcnt lgkmcnt(0)\n\ts_barrier" ::: "memory");
    const __amdgpu_buffer_rsrc_t rws = mk_rsrc(ws, WS_BYTES);
    volatile LAS unsigned* flag = (volatile LAS unsigned*)(L + L2_FLAG);
    const int head = xcd * 4 + (idx & 3), rg = idx >> 2, ww = w - 4, t = tid - 256;
    v4u qf[4];
    const v4u wa0 = __builtin_amdgcn_raw_buffer_load_b128(rws, lane * 16, WPF_OFF + head * 2048, 0);
    const v4u wa1 = __builtin_amdgcn_raw_buffer_load_b128(rws, lane * 16 + 1024, WPF_OFF + head * 2048, 0);
#pragma unroll
    for (int m = 0; m < 4; ++m)
      qf[m] = __builtin_amdgcn_raw_buffer_load_b128(rws, lane * 16 + m * 1024, Q_OFF + (head * 128 + rg * 16 + ww * 4) * 1024, 0);
    f32x4 v[16];
#pragma unroll
    for (int r = 0; r < 16; ++r)
      v[r] = __builtin_bit_cast(f32x4, __builtin_amdgcn_raw_buffer_load_b128(rws, t * 16, KVP_OFF + (head * 16 + r) * KVP_REC, 0));
    SB0();
    STAMP_AT(1, 5);
    v4u fp[4], fn[4];
#pragma unroll
    for (int m = 0; m < 4; ++m) {
      const f32x4 p0 = mfma(wa0, qf[m], z4);
      f32x4 p1 = mfma(wa1, qf[m], z4);
      if (g != 0) p1 = p0;
      float mx = fmaxf(fmaxf(fmaxf(p0[0], p0[1]), fmaxf(p0[2], p0[3])), fmaxf(fmaxf(p1[0], p1[1]), fmaxf(p1[2], p1[3])));
      float mn = fminf(fminf(fminf(p0[0], p0[1]), fminf(p0[2], p0[3])), fminf(fminf(p1[0], p1[1]), fminf(p1[2], p1[3])));
      const float l2e = 1.4426950408889634f;
      const float mxs = red4_max(mx) * -l2e, mns = red4_max(-mn) * -l2e;
      const float keep = g == 0 ? 1.0f : 0.0f;
      f32x2 ep[4], en[4];
#pragma unroll
      for (int i = 0; i < 4; ++i) {
        const f32x4 s = i < 2 ? p0 : p1;
        const f32x2 x = f32x2{s[2 * (i & 1)], s[2 * (i & 1) + 1]};
        ep[i] = exp2v(x * l2e + mxs);
        en[i] = exp2v(x * -l2e + mns);
      }
      const f32x2 sp = (ep[0] + ep[1]) + (ep[2] + ep[3]) * keep, sn = (en[0] + en[1]) + (en[2] + en[3]) * keep;
      const float rp = __builtin_amdgcn_rcpf(red4_sum(sp[0] + sp[1])), rn = __builtin_amdgcn_rcpf(red4_sum(sn[0] + sn[1]));
      const float rpk = rp * keep, rnk = rn * keep;
      fp[m] = v4u{cvtpk(ep[0] * rp), cvtpk(ep[1] * rp), cvtpk(ep[2] * rpk), cvtpk(ep[3] * rpk)};
      fn[m] = v4u{cvtpk(en[0] * rn), cvtpk(en[1] * rn), cvtpk(en[2] * rnk), cvtpk(en[3] * rnk)};
    }
    SB0();
    STAMP_AT(1, 6);
    {
      f32x4 s = v[0];
#pragma unroll
      for (int r = 1; r < 16; ++r) s += v[r];
      int d, f0;
      if (t < 128) { d = (t & 63) >> 2; f0 = 16 * (t >> 6) + 4 * (t & 3); }
      else if (t < 160) { d = (t - 128) >> 1; f0 = 32 + 4 * (t & 1); }
      else if (t < 200) { const int u = t < 180 ? t - 160 : t - 180; d = 16 + (u >> 2); f0 = (t < 180 ? 0 : 16) + 4 * (u & 3); }
      else { d = 16 + ((t - 200) >> 1); f0 = 32 + 4 * (t & 1); }
      if (t < 210) *(LAS v2u*)(L + L2_KVS + d * 80 + f0 * 2) = cvt4(s);
    }
    asm volatile("s_waitcnt lgkmcnt(0)" ::: "memory");
    if (lane == 0) __hip_atomic_fetch_add((LAS unsigned*)(L + L2_FLAG), 1u, __ATOMIC_RELAXED, __HIP_MEMORY_SCOPE_WORKGROUP);
    while (flag[0] < 4u) __builtin_amdgcn_s_sleep(1);
    asm volatile("" ::: "memory");
    STAMP_AT(1, 1);
    {
      v4u ka[2][2];
#pragma unroll
      for (int b = 0; b < 2; ++b)
#pragma unroll
        for (int st = 0; st < 2; ++st) {
          const int d = b == 0 ? tok : (tok < 4 ? 16 + tok : 20);
          LAS char* src = L + L2_KVS + d * 80 + st * 40;
          const v2u lo = *(LAS v2u*)(src + g * 8), hi = *(LAS v2u*)(src + 32);
          ka[b][st] = v4u{lo.x, lo.y, hi.x, hi.y};
        }
      const int rto = rg * 4 + ww;
      const int Q0 = 5 * head + g, Q1 = 5 * head + 4;
      const unsigned o0 = (unsigned)(ACT_OFF + (rto * NKS + NKH + (Q0 >> 3)) * 4096 + (Q0 & 7) * 256 + tok * 16);
      const unsigned o1 = g == 0 ? (unsigned)(ACT_OFF + (rto * NKS + NKH + (Q1 >> 3)) * 4096 + (Q1 & 7) * 256 + tok * 16) : 0x80000000u;
      v2u a[4], b[4];
#pragma unroll
      for (int m = 0; m < 4; ++m) {
        f32x4 n0 = mfma(ka[0][0], fp[m], z4), n1 = mfma(ka[1][0], fp[m], z4);
        n0 = mfma(ka[0][1], fn[m], n0);
        n1 = mfma(ka[1][1], fn[m], n1);
        auto r = __builtin_amdgcn_permlane16_swap(f2u(n1[0]), f2u(n1[0]), false, false);
        const unsigned r1 = r[1];
        const float sc = __builtin_amdgcn_rcpf(u2f(r1) + 1e-6f);
        a[m] = cvt4(n0 * sc); b[m] = cvt4(n1 * sc);
        a[m].x = a[m].x == SENT ? 0x7fc07fc0u : a[m].x; a[m].y = a[m].y == SENT ? 0x7fc07fc0u : a[m].y;
        b[m].x = b[m].x == SENT ? 0x7fc07fc0u : b[m].x; b[m].y = b[m].y == SENT ? 0x7fc07fc0u : b[m].y;
        if (m & 1) {
          st16(rws, o0 + (m >> 1) * 2048, v4u{a[m - 1].x, a[m - 1].y, a[m].x, a[m].y});
          st16(rws, o1 + (m >> 1) * 2048, v4u{b[m - 1].x, b[m - 1].y, b[m].x, b[m].y});
        }
      }
    }
    STAMP_AT(1, 2);
    v4u wa[5][5];
#pragma unroll
    for (int i = 0; i < 5; ++i)
#pragma unroll
      for (int nb = 0; nb < 5; ++nb) wa[i][nb] = __builtin_amdgcn_raw_buffer_load_b128(rw, lane * 16 + nb * 1024, (NKH + 5 * ww + i) * 5120, 0);
    SB0();
    {
      const int qd = lane < 40 ? lane : 39;
      const unsigned co = (unsigned)((qd >> 3) * 4096 + 2048 + (qd & 7) * 256 + 252);
      for (unsigned spins = 0; spins < (1u << 16); ++spins) {
        const unsigned cv = __builtin_amdgcn_raw_buffer_load_b32(ra, co, (NKH + 5 * ww) * 4096, 16);
        if (!__any(cv == SENT)) break;
        __builtin_amdgcn_s_sleep(4);
      }
    }
    STAMP_AT(1, 7);
    v4u ob[5][4];
    for (unsigned spins = 0;; ++spins) {
#pragma unroll
      for (int i = 0; i < 5; ++i)
#pragma unroll
        for (int m = 0; m < 4; ++m) ob[i][m] = __builtin_amdgcn_raw_buffer_load_b128(ra, lane * 16 + m * 1024, (NKH + 5 * ww + i) * 4096, OLD_AUX);
      bool bad = false;
#pragma unroll
      for (int i = 0; i < 5; ++i)
#pragma unroll
        for (int m = 0; m < 4; ++m)
          bad |= (ob[i][m].x == SENT) | (ob[i][m].y == SENT) | (ob[i][m].z == SENT) | (ob[i][m].w == SENT);
      if (!__any(bad) || spins > (1u << 12)) break;
      __builtin_amdgcn_s_sleep(8);
    }
    STAMP_AT(1, 3);
#pragma unroll
    for (int i = 0; i < 5; ++i) {
      v4u fr[4];
#pragma unroll
      for (int pr = 0; pr < 2; ++pr) {
        fr[2 * pr] = v4u{ob[i][2 * pr].x, ob[i][2 * pr].y, ob[i][2 * pr + 1].x, ob[i][2 * pr + 1].y};
        fr[2 * pr + 1] = v4u{ob[i][2 * pr].z, ob[i][2 * pr].w, ob[i][2 * pr + 1].z, ob[i][2 * pr + 1].w};
      }
#pragma unroll
      for (int nb = 0; nb < 5; ++nb)
#pragma unroll
        for (int m = 0; m < 4; ++m) acc[nb][m] = mfma(wa[i][nb], fr[m], acc[nb][m]);
    }
#pragma unroll
    for (int m = 0; m < 4; ++m)
#pragma unroll
      for (int nb = 0; nb < 5; ++nb) *(LAS f32x4*)(L + L2_REDO + ((m * 4 + ww) * 5 + nb) * 1024 + lane * 16) = acc[nb][m];
    STAMP_AT(1, 4);
    __syncthreads();
    STAMP_FLUSH(1);
    return;
  }

  unsigned vb[4];
#pragma unroll
  for (int i = 0; i < 4; ++i) vb[i] = lane * 16 + ((w + i) & 3) * 1024;
  asm volatile("s_barrier" ::: "memory");
  STAMP_AT(1, 5);
  const float gsc = *((const float*)(ws + G_OFF) + rt64 * 64 + w * 16 + tok);
  if (w < 2) k2_gemm<14>(acc, ra, rw, vb, lane * 16, 14 * w);
  else k2_gemm<13>(acc, ra, rw, vb, lane * 16, 28 + 13 * (w - 2));
  STAMP_AT(1, 1);
#pragma unroll
  for (int i = 1; i < 4; ++i) {
    LAS char* dst = L + L2_RED + (((w + i) & 3) * 3 + (i - 1)) * 5120 + lane * 16;
#pragma unroll
    for (int nb = 0; nb < 5; ++nb) *(LAS f32x4*)(dst + nb * 1024) = acc[nb][i];
  }
  __syncthreads();
  STAMP_AT(1, 2);
  f32x4 r[5];
#pragma unroll
  for (int nb = 0; nb < 5; ++nb) r[nb] = acc[nb][0];
#pragma unroll
  for (int sl = 0; sl < 3; ++sl)
#pragma unroll
    for (int nb = 0; nb < 5; ++nb) r[nb] += *(LAS f32x4*)(L + L2_RED + ((w * 3 + sl) * 5 + nb) * 1024 + lane * 16);
  f32x4 ro[5];
#pragma unroll
  for (int nb = 0; nb < 5; ++nb) ro[nb] = *(LAS f32x4*)(L + L2_REDO + ((w * 4) * 5 + nb) * 1024 + lane * 16);
#pragma unroll
  for (int sl = 1; sl < 4; ++sl)
#pragma unroll
    for (int nb = 0; nb < 5; ++nb) ro[nb] += *(LAS f32x4*)(L + L2_REDO + ((w * 4 + sl) * 5 + nb) * 1024 + lane * 16);
#pragma unroll
  for (int nb = 0; nb < 5; ++nb) r[nb] = r[nb] * (1.0f - gsc) + ro[nb] * gsc;
  u16* op = p.out + (rt64 * 64 + w * 16 + tok) * D_ + ct * 80;
  *(v4u*)(op + 8 * g) = cvt8(r[0], r[1]);
  *(v4u*)(op + 32 + 8 * g) = cvt8(r[2], r[3]);
  *(v2u*)(op + 64 + 4 * g) = cvt4(r[4]);
  STAMP_AT(1, 3);
  STAMP_FLUSH(1);
}

extern "C" void kernel_launch(void* const* d_in, const int* in_sizes, int n_in, void* d_out, int out_size, void* d_ws,
                              size_t ws_size, hipStream_t stream) {
  K1Args a;
  a.x = (const u16*)d_in[0]; a.wqkv = (const u16*)d_in[1]; a.wphi = (const u16*)d_in[2]; a.wo = (const u16*)d_in[3];
  a.wg = (const u16*)d_in[5]; a.wu = (const u16*)d_in[6]; a.wd = (const u16*)d_in[7]; a.gw = (const u16*)d_in[8];
  a.ws = (char*)d_ws;
  K2Args b;
  b.ws = a.ws; b.out = (u16*)d_out;
  (void)hipFuncSetAttribute((const void*)k1, hipFuncAttributeMaxDynamicSharedMemorySize, K1_LDS);
  (void)hipFuncSetAttribute((const void*)k2, hipFuncAttributeMaxDynamicSharedMemorySize, K2_LDS);
  k1<<<256, 768, K1_LDS, stream>>>(a);
  k2<<<256, 512, K2_LDS, stream>>>(b);
}
```

```cpp
#include <hip/hip_runtime.h>

typedef unsigned short u16;
typedef unsigned v4u __attribute__((ext_vector_type(4)));
typedef unsigned v2u __attribute__((ext_vector_type(2)));
typedef float f32x4 __attribute__((ext_vector_type(4)));
typedef float f32x2 __attribute__((ext_vector_type(2)));
typedef short s16x4 __attribute__((ext_vector_type(4)));
typedef __bf16 bf16x8 __attribute__((ext_vector_type(8)));
typedef __bf16 bf16x2 __attribute__((ext_vector_type(2)));
#define LAS __attribute__((address_space(3)))
#define GAS __attribute__((address_space(1)))
#define DI __device__ __forceinline__

constexpr int S_ = 2048, D_ = 640, F_ = 1728;
constexpr int NKS = 74, NKH = 54;
constexpr unsigned ACT_OFF = 0, ACT_BYTES = 32u * NKS * 4 * 1024;
constexpr unsigned WT_OFF = ACT_OFF + ACT_BYTES, WT_BYTES = 8u * NKS * 5 * 1024;
constexpr unsigned Q_OFF = WT_OFF + WT_BYTES, Q_BYTES = 32u * 128 * 1024;
constexpr unsigned KVP_REC = 3360;
constexpr unsigned KVP_OFF = Q_OFF + Q_BYTES, KVP_BYTES = 32u * 16 * KVP_REC;
constexpr unsigned G_OFF = KVP_OFF + KVP_BYTES, G_BYTES = S_ * 4;
constexpr unsigned WPF_OFF = G_OFF + G_BYTES, WPF_BYTES = 32u * 2 * 1024;
constexpr unsigned WS_BYTES = WPF_OFF + WPF_BYTES;
constexpr unsigned SENT = 0xFFFFFFFFu;

constexpr unsigned L_W = 0, WSLOT = 24576, L_X = 4 * WSLOT, XSLOT = 16384, L_WPHI = L_X + 2 * XSLOT, L_ZERO = L_WPHI + 6400,
                   L_DUMMY = L_WPHI + 8192, K1_LDS = 163840;
constexpr unsigned L_PK = 0, L_V = 8 * 128 * 80;

DI unsigned rfl(unsigned x) { return __builtin_amdgcn_readfirstlane(x); }
DI unsigned cvtpk(f32x2 v) {
  bf16x2 b = __builtin_convertvector(v, bf16x2);
  return __builtin_bit_cast(unsigned, b);
}
DI unsigned cvtpk(float lo, float hi) { return cvtpk(f32x2{lo, hi}); }
DI v2u cvt4(f32x4 v) { return v2u{cvtpk(v[0], v[1]), cvtpk(v[2], v[3])}; }
DI v4u cvt8(f32x4 a, f32x4 b) { return v4u{cvtpk(a[0], a[1]), cvtpk(a[2], a[3]), cvtpk(b[0], b[1]), cvtpk(b[2], b[3])}; }
DI float bf2f(unsigned b) { return __builtin_bit_cast(float, b << 16); }
DI f32x4 mfma(v4u a, v4u b, f32x4 c) {
  return __builtin_amdgcn_mfma_f32_16x16x32_bf16(__builtin_bit_cast(bf16x8, a), __builtin_bit_cast(bf16x8, b), c, 0, 0, 0);
}
DI v2u tr_read(LAS char* p) {
  s16x4 r = __builtin_amdgcn_ds_read_tr16_b64_v4i16((LAS s16x4*)p);
  return __builtin_bit_cast(v2u, r);
}
DI v4u tr_read2(LAS char* p, int d) {
  v2u a = tr_read(p), b = tr_read(p + d);
  return v4u{a.x, a.y, b.x, b.y};
}
DI __amdgpu_buffer_rsrc_t mk_rsrc(const void* p, unsigned bytes) {
  return __builtin_amdgcn_make_buffer_rsrc((void*)p, (short)0, (int)bytes, 0x00020000);
}
DI float u2f(unsigned u) { return __builtin_bit_cast(float, u); }
DI unsigned f2u(float f) { return __builtin_bit_cast(unsigned, f); }
DI float red4_max(float x) {
  auto r = __builtin_amdgcn_permlane16_swap(f2u(x), f2u(x), false, false);
  unsigned r0 = r[0], r1 = r[1];
  x = fmaxf(u2f(r0), u2f(r1));
  auto t = __builtin_amdgcn_permlane32_swap(f2u(x), f2u(x), false, false);
  r0 = t[0]; r1 = t[1];
  return fmaxf(u2f(r0), u2f(r1));
}
DI float red4_sum(float x) {
  auto r = __builtin_amdgcn_permlane16_swap(f2u(x), f2u(x), false, false);
  unsigned r0 = r[0], r1 = r[1];
  x = u2f(r0) + u2f(r1);
  auto t = __builtin_amdgcn_permlane32_swap(f2u(x), f2u(x), false, false);
  r0 = t[0]; r1 = t[1];
  return u2f(r0) + u2f(r1);
}
DI f32x2 exp2v(f32x2 a) { return f32x2{__builtin_amdgcn_exp2f(a[0]), __builtin_amdgcn_exp2f(a[1])}; }
DI void st16(__amdgpu_buffer_rsrc_t r, unsigned off, v4u v) { __builtin_amdgcn_raw_buffer_store_b128(v, r, off, 0, 16); }
DI void st8(__amdgpu_buffer_rsrc_t r, unsigned off, v2u v) { __builtin_amdgcn_raw_buffer_store_b64(v, r, off, 0, 16); }
#define PF_MODE 1
#define SB0() __builtin_amdgcn_sched_barrier(0)
#define SYNC(n) do { SB0(); asm volatile("s_waitcnt vmcnt(" #n ") lgkmcnt(0)\n\ts_barrier" ::: "memory"); SB0(); } while (0)
#define LDSDMA(m0v, vo, rs, so) \
  asm volatile("s_mov_b32 m0, %0\n\ts_nop 0\n\tbuffer_load_dwordx4 %1, %2, %3 offen lds" :: "s"(m0v), "v"(vo), "s"(rs), "s"(so) : "memory")

#define STAMP_DECL
#define STAMP_AT(kern, i)
#define STAMP_FLUSH(kern)
#define STAMP_ARG
#define STAMP_PASS

struct K1Args {
  const u16 *x, *wqkv, *wphi, *wo, *wg, *wu, *wd, *gw;
  char* ws;
};
struct K2Args {
  char* ws;
  u16* out;
};

#define SYNCL() do { SB0(); asm volatile("s_waitcnt lgkmcnt(0)\n\ts_barrier" ::: "memory"); SB0(); } while (0)
#define DMA(m0v, vo, rs, so, off) \
  asm volatile("s_mov_b32 m0, %0\n\ts_nop 0\n\tbuffer_load_dwordx4 %1, %2, %3 offen offset:%4 lds" :: "s"(m0v), "v"(vo), "s"(rs), "s"(so), "i"(off) : "memory")

template <bool GU, int B> DI LAS char* a_addr(LAS char* wbase, const unsigned (&aoff)[5]) {
  return wbase + aoff[GU ? B >> 1 : B] + ((GU && (B & 1)) ? 12288 : 0);
}
DI void mfma_ip(f32x4& c, const v4u& a, const v4u& b) {
  asm volatile("v_mfma_f32_16x16x32_bf16 %0, %1, %2, %0" : "+v"(c) : "v"(a), "v"(b));
}
template <bool GU, bool NEXT, int T, int J = 0>
DI void k1_step(f32x4 (&acc)[4][GU ? 6 : 5], v4u (&a)[4], v4u (&x)[2][4], LAS char* L, const unsigned (&aoff)[5],
                const unsigned (&xoff)[2]) {
  constexpr int NB = GU ? 6 : 5, R0 = T * NB;
  if constexpr (J < 4 * NB) {
    constexpr int b = J >> 2, r = J & 3, nb = b + 2;
    if constexpr (r < 2) {
      if constexpr (nb < NB || NEXT) {
        v2u t = tr_read(a_addr<GU, nb % NB>(L + L_W + ((nb < NB ? T : T + 1) & 3) * WSLOT, aoff) + r * 1536);
        if constexpr (r == 0) { a[(R0 + nb) & 3].x = t.x; a[(R0 + nb) & 3].y = t.y; }
        else { a[(R0 + nb) & 3].z = t.x; a[(R0 + nb) & 3].w = t.y; }
      }
    } else if constexpr (r == 2 && b < 4 && NEXT)
      x[(T + 1) & 1][b] = *(LAS v4u*)(L + L_X + (((T + 1) >> 1) & 1) * XSLOT + xoff[(T + 1) & 1] + b * 2048);
    mfma_ip(acc[r][b], a[(R0 + b) & 3], x[T & 1][r]);
    SB0();
    k1_step<GU, NEXT, T, J + 1>(acc, a, x, L, aoff, xoff);
  }
}
template <bool GU>
DI void k1_mainloop(const K1Args& p, f32x4 (&acc)[4][GU ? 6 : 5], LAS char* L, const unsigned (&aoff)[5], unsigned xoff0 STAMP_ARG) {
  const unsigned xoff[2] = {xoff0, xoff0 ^ 64};
  v4u a[4], x[2][4];
  SYNCL();
  STAMP_AT(0, 4);
#pragma unroll
  for (int m = 0; m < 4; ++m) x[0][m] = *(LAS v4u*)(L + L_X + xoff0 + m * 2048);
  a[0] = tr_read2(a_addr<GU, 0>(L + L_W, aoff), 1536);
  a[1] = tr_read2(a_addr<GU, 1>(L + L_W, aoff), 1536);
#pragma unroll 1
  for (int i = 0; i < 16; i += 4) {
    SYNCL(); k1_step<GU, true, 0>(acc, a, x, L, aoff, xoff);
    SYNCL(); k1_step<GU, true, 1>(acc, a, x, L, aoff, xoff);
    SYNCL(); k1_step<GU, true, 2>(acc, a, x, L, aoff, xoff);
    SYNCL(); k1_step<GU, true, 3>(acc, a, x, L, aoff, xoff);
  }
  SYNCL(); k1_step<GU, true, 0>(acc, a, x, L, aoff, xoff);
  SYNCL(); k1_step<GU, true, 1>(acc, a, x, L, aoff, xoff);
  SYNCL(); k1_step<GU, true, 2>(acc, a, x, L, aoff, xoff);
  k1_step<GU, false, 3>(acc, a, x, L, aoff, xoff);
  asm volatile("s_nop 15" ::: "memory");
  SB0();
}

DI unsigned gelu_mul2(f32x2 x, f32x2 u) {
  const float k0 = -2.0f * 0.7978845608028654f * 1.4426950408889634f, k1 = k0 * 0.044715f;
  f32x2 t = x * x;
  f32x2 a = x * (t * k1 + k0);
  f32x2 d = exp2v(a) + 1.0f;
  f32x2 r = f32x2{__builtin_amdgcn_rcpf(d[0]), __builtin_amdgcn_rcpf(d[1])};
  return cvtpk((x * u) * r);
}
DI v2u gelu_mul4(f32x4 x, f32x4 u) {
  return v2u{gelu_mul2(f32x2{x[0], x[1]}, f32x2{u[0], u[1]}), gelu_mul2(f32x2{x[2], x[3]}, f32x2{u[2], u[3]})};
}

__global__ __launch_bounds__(768, 3) void k1(K1Args p) {
  extern __shared__ __attribute__((aligned(16))) char smem[];
  LAS char* L = (LAS char*)smem;
  const int tid = threadIdx.x, lane = tid & 63, w = rfl(tid >> 6);
  const int bid = blockIdx.x, xcd = bid & 7, idx = bid >> 3;
  const unsigned long long CSMAP = 0xDC87BA65E943F210ull;
  const int rt = (xcd & 1) * 8 + (idx & 7), cs = (int)(CSMAP >> (4 * ((xcd >> 1) * 4 + (idx >> 3)))) & 15;
  const int g = lane >> 4, q = (lane >> 2) & 3, pp = lane & 3, tok = lane & 15;
  char* ws = p.ws;
  const __amdgpu_buffer_rsrc_t rws = mk_rsrc(ws, WS_BYTES);
  STAMP_DECL;
  STAMP_AT(0, 0);

  if (cs == 15) {
    if (w >= 8) return;
    const __amdgpu_buffer_rsrc_t rd = mk_rsrc(p.wd, F_ * D_ * 2), ro = mk_rsrc(p.wo, D_ * D_ * 2);
    unsigned rowi[5], chb[5];
#pragma unroll
    for (int j = 0; j < 5; ++j) {
      const int Lc = (w * 5 + j) * 64 + lane;
      rowi[j] = Lc / 80; chb[j] = (Lc % 80) * 16;
    }
#pragma unroll 1
    for (int round = 0; round < 2; ++round) {
      const int nsl = round == 0 ? 3 : (rt + 64 < NKS ? 2 : 1);
#pragma unroll 1
      for (int sl = 0; sl < nsl; ++sl) {
        const int s = rt + 16 * (round * 3 + sl);
        int b0, b1;
        if (s < NKH) {
          int u = s / 6, e = s % 6;
          if (e < 4) { b0 = 192 * u + 48 * e; b1 = b0 + 16; } else { b0 = 192 * u + 96 * (e - 4) + 32; b1 = b0 + 48; }
        } else { b0 = 32 * (s - NKH); b1 = b0 + 16; }
        const __amdgpu_buffer_rsrc_t rs = s < NKH ? rd : ro;
#pragma unroll
        for (int j = 0; j < 5; ++j) {
          const unsigned vo = (rowi[j] < 16 ? b0 + rowi[j] : b1 + rowi[j] - 16) * 1280 + chb[j];
          const unsigned m0 = (unsigned)(sl * 40960 + (w * 5 + j) * 1024);
          LDSDMA(m0, vo, rs, 0);
        }
      }
      if (round == 0) {
        v4u gwv[5], xv[4][5];
#pragma unroll
        for (int i = 0; i < 5; ++i) gwv[i] = *(const v4u*)(p.gw + (tok + 16 * i) * 8);
#pragma unroll
        for (int it = 0; it < 4; ++it)
#pragma unroll
          for (int i = 0; i < 5; ++i) xv[it][i] = *(const v4u*)(p.x + (rt * 128 + w * 16 + it * 4 + g) * D_ + (tok + 16 * i) * 8);
        const int head = 2 * rt + ((w >> 1) & 1), f = 16 * (w & 1) + tok;
        const u16* wp = p.wphi + head * 400 + (f < 20 ? f : 0);
        unsigned e[5];
#pragma unroll
        for (int j = 0; j < 4; ++j) e[j] = wp[(4 * g + j) * 20];
        e[4] = wp[(16 + g) * 20];
        float a[4];
#pragma unroll
        for (int it = 0; it < 4; ++it) {
          float s0 = 0.f, s1 = 0.f;
#pragma unroll
          for (int i = 0; i < 5; ++i)
#pragma unroll
            for (int k = 0; k < 4; ++k) {
              const unsigned xe = xv[it][i][k], ge = gwv[i][k];
              s0 = __builtin_fmaf(bf2f(xe & 0xffffu), bf2f(ge & 0xffffu), s0);
              s1 = __builtin_fmaf(u2f(xe & 0xffff0000u), u2f(ge & 0xffff0000u), s1);
            }
          float s = s0 + s1;
          s += __shfl_xor(s, 1); s += __shfl_xor(s, 2); s += __shfl_xor(s, 4); s += __shfl_xor(s, 8);
          a[it] = s;
        }
        const float av = tok == 0 ? a[0] : tok == 1 ? a[1] : tok == 2 ? a[2] : a[3];
        if (tok < 4)
          __builtin_amdgcn_raw_buffer_store_b32(f2u(1.0f / (1.0f + __expf(-av))), rws, G_OFF + (rt * 128 + w * 16 + tok * 4 + g) * 4, 0, 16);
        v4u fr = v4u{e[0] | e[1] << 16, e[2] | e[3] << 16, e[4], 0u};
        if (f >= 20) fr = v4u{0u, 0u, 0u, 0u};
        if (w < 4) st16(rws, WPF_OFF + (head * 2 + (w & 1)) * 1024 + lane * 16, fr);
      }
      SYNC(0);
#pragma unroll 1
      for (int sl = 0; sl < nsl; ++sl) {
        const int s = rt + 16 * (round * 3 + sl);
        LAS char* base = L + sl * 40960 + (4 * g + q) * 1280 + w * 160;
#pragma unroll
        for (int nb = 0; nb < 5; ++nb) {
          const int col = nb < 4 ? 32 * (nb >> 1) + 8 * pp + 4 * (nb & 1) : 64 + 4 * pp;
          v4u fr = tr_read2(base + col * 2, 16 * 1280);
          st16(rws, WT_OFF + ((w * NKS + s) * 5 + nb) * 1024 + lane * 16, fr);
        }
      }
      asm volatile("s_waitcnt lgkmcnt(0)\n\ts_barrier" ::: "memory");
      if (round == 0) STAMP_AT(0, 1); else STAMP_AT(0, 2);
    }
    STAMP_FLUSH(0);
    return;
  }

  const bool isGU = cs < 9, isKV = cs >= 11;
  const __amdgpu_buffer_rsrc_t rx = mk_rsrc(p.x + rt * 128 * D_, (unsigned)((S_ - rt * 128) * D_ * 2));
  const unsigned pitch = isGU ? F_ * 2 : 3 * D_ * 2, total = D_ * pitch;
  unsigned boffA, boffB;
  if (isGU) boffA = boffB = 384u * cs;
  else if (!isKV) { boffA = 640u * (cs - 9); boffB = boffA + 256u; }
  else { const int j = cs - 11; boffA = 1280u + 320u * j - ((j & 1) ? 64u : 0u); boffB = boffA + 1280u; }
  const char* baseA = (const char*)(isGU ? p.wg : p.wqkv) + boffA;
  const char* baseB = (const char*)(isGU ? p.wu : p.wqkv) + boffB;
  if (w >= 8) {
    __builtin_amdgcn_s_setprio(3);
    const int lw = w - 8, sub = lw >> 1;
    const int r0 = 32 * lw + (lane >> 3);
    const unsigned vx0 = (unsigned)(r0 * 1280 + (((lane & 7) ^ ((r0 >> 1) & 7)) * 16)), vx1 = (vx0 ^ 64u) + 8 * 1280 - 1024;
    const unsigned m0x = L_X + lw * 4096;
#define LOAD_X(stage, slot, e) do { const unsigned m0_ = m0x + (unsigned)(slot) * XSLOT + (e) * 2048, so_ = (unsigned)(stage) * 128 + (e) * 16 * 1280; \
      DMA(m0_, vx0, rx, so_, 0); DMA(m0_, vx1, rx, so_, 1024); } while (0)
    LOAD_X(0, 0, 0); LOAD_X(0, 0, 1);
    const __amdgpu_buffer_rsrc_t rw = mk_rsrc((sub ? baseB : baseA) + (lw & 1) * 16 * pitch, total - (sub ? boffB : boffA) - (lw & 1) * 16 * pitch);
    unsigned vw[6];
#pragma unroll
    for (int j = 0; j < 3; ++j) {
      const int Lc = 64 * j + lane, kr = Lc / 24, pos = Lc % 24;
      const unsigned c0 = (unsigned)((pos >> 3) * 128 + (((pos & 7) ^ (kr & 2)) * 16));
      vw[j] = (unsigned)kr * pitch + c0 - 1024u * j;
      vw[3 + j] = (unsigned)(kr + 8) * pitch + (c0 ^ 64u) - 1024u * j;
    }
    const unsigned m0w = L_W + sub * 12288 + (lw & 1) * 6144, wstep = 32 * pitch;
#define LOAD_W(s) do { const unsigned m0_ = m0w + (unsigned)((s) & 3) * WSLOT, so_ = (unsigned)(s) * wstep; \
      DMA(m0_, vw[0], rw, so_, 0); DMA(m0_, vw[1], rw, so_, 1024); DMA(m0_, vw[2], rw, so_, 2048); \
      DMA(m0_ + 3072, vw[3], rw, so_, 0); DMA(m0_ + 3072, vw[4], rw, so_, 1024); DMA(m0_ + 3072, vw[5], rw, so_, 2048); } while (0)
    LOAD_W(0);
    LOAD_W(1); LOAD_X(1, 1, 0); LOAD_X(1, 1, 1);
    LOAD_W(2);
    SYNC(16);
    SYNC(6); LOAD_W(3);
#pragma unroll 1
    for (int i = 1; i < 15; i += 2) {
      SYNC(6); LOAD_X((i + 3) >> 1, ((i + 3) >> 1) & 1, 0); LOAD_X((i + 3) >> 1, ((i + 3) >> 1) & 1, 1); LOAD_W(i + 3);
      SYNC(10); LOAD_W(i + 4);
    }
    SYNC(6); LOAD_X(9, 1, 0); LOAD_X(9, 1, 1); LOAD_W(18);
    SYNC(10); LOAD_W(19);
    if (isKV) {
      const __amdgpu_buffer_rsrc_t rp = mk_rsrc(p.wphi + 8 * (cs - 11) * 400, 6400);
      DMA(L_WPHI + lw * 2048, (unsigned)(lw * 2048 + lane * 16), rp, 0, 0);
      DMA(L_WPHI + lw * 2048, (unsigned)(lw * 2048 + lane * 16), rp, 0, 1024);
    }
    SYNC(6);
    SYNC(0);
    return;
  }
  {
    constexpr int NSH = PF_MODE == 1 ? 16 : 8, RPB = 64 / NSH;
    const int sh = rt & (NSH - 1);
    const __amdgpu_buffer_rsrc_t rA = mk_rsrc(baseA, total - boffA), rB = mk_rsrc(baseB, total - boffB);
    const unsigned so = (unsigned)(64 * (w + 2) + RPB * sh) * pitch;
#pragma unroll
    for (int j = 0; j < RPB * 24 / 64 + (RPB * 24 % 64 ? 1 : 0); ++j) {
      const int Lc = 64 * j + lane;
      const unsigned vo = Lc < RPB * 24 ? (unsigned)(Lc / 24) * pitch + (unsigned)(Lc % 24) * 16 : 0x80000000u;
      DMA(L_DUMMY + w * 1024, vo, rA, so, 0);
      DMA(L_DUMMY + w * 1024, vo, rB, so, 0);
    }
    constexpr int NXI = PF_MODE == 1 ? 1 : 4;
    const int xi = PF_MODE == 1 ? cs : 4 * (idx >> 3);
#pragma unroll
    for (int j = 0; j < NXI; ++j)
      DMA(L_DUMMY + w * 1024, (unsigned)((8 * (xi + j) + (lane >> 3)) * 1280 + (lane & 7) * 16), rx, (unsigned)(w + 2) * 128, 0);
  }
  const int mh = w & 1, n = w >> 1;
  const int sw = ((q >> 1) & 1) | ((g & 1) << 1);
  const unsigned xoff0 = (unsigned)((64 * mh + tok) * 128 + ((g ^ (tok >> 1)) * 16));
  const int rt64 = rt * 2 + mh;

  if (isGU) {
    unsigned aoff[5];
#pragma unroll
    for (int b = 0; b < 3; ++b) {
      const int ch = 6 * n + 2 * b + (pp >> 1);
      aoff[b] = (unsigned)((8 * g + q) * 384 + (ch >> 3) * 128 + (((ch & 7) ^ (2 * sw)) * 16) + (pp & 1) * 8);
    }
    aoff[3] = aoff[4] = 0;
    f32x4 acc[4][6];
#pragma unroll
    for (int m = 0; m < 4; ++m)
#pragma unroll
      for (int b = 0; b < 6; ++b) acc[m][b] = f32x4{0.f, 0.f, 0.f, 0.f};
    k1_mainloop<true>(p, acc, L, aoff, xoff0 STAMP_PASS);
    STAMP_AT(0, 1);
    const unsigned actA = ACT_OFF + ((rt64 * NKS + 6 * cs + n) * 4) * 1024 + lane * 16;
    const unsigned actB = ACT_OFF + ((rt64 * NKS + 6 * cs + 4 + (n >> 1)) * 4 + 2 * (n & 1)) * 1024 + lane * 16;
    v2u h2[4];
#pragma unroll
    for (int m = 0; m < 4; ++m) h2[m] = gelu_mul4(acc[m][4], acc[m][5]);
    const bool odd = n & 1;
    *(LAS v4u*)(L + L_WPHI + w * 1024 + lane * 16) = odd ? v4u{h2[0].x, h2[0].y, h2[1].x, h2[1].y} : v4u{h2[2].x, h2[2].y, h2[3].x, h2[3].y};
    const v2u k0 = odd ? h2[2] : h2[0], k1 = odd ? h2[3] : h2[1];
#pragma unroll
    for (int m = 0; m < 4; ++m) {
      const v2u h0 = gelu_mul4(acc[m][0], acc[m][1]), h1 = gelu_mul4(acc[m][2], acc[m][3]);
      st16(rws, actA + m * 1024, v4u{h0.x, h0.y, h1.x, h1.y});
      if (m == 1) {
        SYNCL();
        const v4u o = *(LAS v4u*)(L + L_WPHI + (w ^ 2) * 1024 + lane * 16);
        st16(rws, actB, odd ? v4u{o.x, o.y, k0.x, k0.y} : v4u{k0.x, k0.y, o.x, o.y});
        st16(rws, actB + 1024, odd ? v4u{o.z, o.w, k1.x, k1.y} : v4u{k1.x, k1.y, o.z, o.w});
      }
    }
    STAMP_AT(0, 2);
    STAMP_FLUSH(0);
    return;
  }

  unsigned aoff[5];
  {
    const int offch = isKV ? ((cs - 11) & 1) * 4 : (n >> 1) * 4;
#pragma unroll
    for (int b = 0; b < 5; ++b) {
      const int u = 2 * offch + 20 * (n & 1) + 5 * pp + b, ch = u >> 1;
      aoff[b] = (unsigned)((n >> 1) * 12288 + (8 * g + q) * 384 + (ch >> 3) * 128 + (((ch & 7) ^ (2 * sw)) * 16) + (u & 1) * 8);
    }
  }
  f32x4 acc[4][5];
#pragma unroll
  for (int m = 0; m < 4; ++m)
#pragma unroll
    for (int b = 0; b < 5; ++b) acc[m][b] = f32x4{0.f, 0.f, 0.f, 0.f};
  k1_mainloop<false>(p, acc, L, aoff, xoff0 STAMP_PASS);
  STAMP_AT(0, 1);

  if (!isKV) {
    const int head = 16 * (cs - 9) + 4 * n + g;
    const unsigned qp = Q_OFF + (head * 128 + rt64 * 4) * 1024 + tok * 16;
#pragma unroll
    for (int m = 0; m < 4; ++m) {
      const v2u c4 = cvt4(acc[m][4]);
#pragma unroll
      for (int b = 0; b < 4; ++b) {
        const v2u c = cvt4(acc[m][b]);
        const unsigned e = b < 2 ? c4.x : c4.y;
        st16(rws, qp + m * 1024 + b * 256, v4u{c.x, c.y, (b & 1) ? e >> 16 : e & 0xffffu, 0u});
      }
    }
    const unsigned op = ACT_OFF + ((2 * rt + cs - 9) * NKS + NKH) * 4096 + tid * 16;
#pragma unroll
    for (int i = 0; i < 10; ++i) st16(rws, op + i * 8192, v4u{SENT, SENT, SENT, SENT});
    STAMP_AT(0, 2);
    STAMP_FLUSH(0);
    return;
  }

  const int hl = 4 * (n & 1) + g;
  v4u c01[4], c23[4];
  v2u c4[4];
#pragma unroll
  for (int m = 0; m < 4; ++m) { c01[m] = cvt8(acc[m][0], acc[m][1]); c23[m] = cvt8(acc[m][2], acc[m][3]); c4[m] = cvt4(acc[m][4]); }
  SYNCL();
  if (n < 2) {
    v4u wf[5][3];
    LAS char* wb = L + (pp == g ? L_WPHI + (unsigned)(hl * 800 + q * 40) : L_ZERO);
#pragma unroll
    for (int bo = 0; bo < 5; ++bo)
#pragma unroll
      for (int s3 = 0; s3 < 3; ++s3) {
        v2u lo = tr_read(wb + s3 * 320 + bo * 8), hi = v2u{0u, 0u};
        if (s3 < 2) hi = tr_read(wb + s3 * 320 + 160 + bo * 8);
        wf[bo][s3] = v4u{lo.x, lo.y, hi.x, hi.y};
      }
    LAS char* dst0 = L + L_PK + (hl * 128 + 64 * mh + tok) * 80;
#pragma unroll
    for (int m = 0; m < 4; ++m) {
      const v4u kf[3] = {c01[m], c23[m], v4u{c4[m].x, c4[m].y, 0u, 0u}};
      f32x4 pk[5];
#pragma unroll
      for (int bo = 0; bo < 5; ++bo) {
        pk[bo] = f32x4{0.f, 0.f, 0.f, 0.f};
#pragma unroll
        for (int s3 = 0; s3 < 3; ++s3) pk[bo] = mfma(wf[bo][s3], kf[s3], pk[bo]);
      }
      float mx = pk[0][0], mn = pk[0][0];
#pragma unroll
      for (int bo = 0; bo < 5; ++bo)
#pragma unroll
        for (int r = 0; r < 4; ++r) { mx = fmaxf(mx, pk[bo][r]); mn = fminf(mn, pk[bo][r]); }
      const float l2e = 1.4426950408889634f;
      const float mxs = -mx * l2e, mns = mn * l2e;
      f32x2 ep[10], en[10], sp = {0.f, 0.f}, sn = {0.f, 0.f};
#pragma unroll
      for (int i = 0; i < 10; ++i) {
        const f32x2 v = f32x2{pk[i >> 1][2 * (i & 1)], pk[i >> 1][2 * (i & 1) + 1]};
        ep[i] = exp2v(v * l2e + mxs);
        en[i] = exp2v(v * -l2e + mns);
        sp += ep[i]; sn += en[i];
      }
      const float rp = __builtin_amdgcn_rcpf(sp[0] + sp[1]), rn = __builtin_amdgcn_rcpf(sn[0] + sn[1]);
      unsigned o[20];
#pragma unroll
      for (int i = 0; i < 10; ++i) { o[i] = cvtpk(ep[i] * rp); o[10 + i] = cvtpk(en[i] * rn); }
      LAS char* dst = dst0 + m * 16 * 80;
#pragma unroll
      for (int i = 0; i < 5; ++i) *(LAS v4u*)(dst + i * 16) = v4u{o[4 * i], o[4 * i + 1], o[4 * i + 2], o[4 * i + 3]};
    }
  } else {
#pragma unroll
    for (int m = 0; m < 4; ++m) {
      LAS char* dst = L + L_V + (hl * 128 + 64 * mh + 16 * m + tok) * 48;
      *(LAS v4u*)(dst) = c01[m];
      *(LAS v4u*)(dst + 16) = c23[m];
      *(LAS v4u*)(dst + 32) = v4u{c4[m].x, c4[m].y, 0x00003f80u, 0u};
    }
  }
  STAMP_AT(0, 2);
  __syncthreads();
  {
    f32x4 kv[3][2];
#pragma unroll
    for (int fb = 0; fb < 3; ++fb) { kv[fb][0] = f32x4{0.f, 0.f, 0.f, 0.f}; kv[fb][1] = f32x4{0.f, 0.f, 0.f, 0.f}; }
    LAS char* pa = L + L_PK + (w * 128 + 8 * g + q) * 80 + pp * 8;
    LAS char* pb = L + L_V + (w * 128 + 8 * g + q) * 48 + pp * 8;
#pragma unroll
    for (int ks = 0; ks < 4; ++ks) {
      v4u af[3], bf[2];
#pragma unroll
      for (int fb = 0; fb < 3; ++fb) af[fb] = tr_read2(pa + ks * 32 * 80 + fb * 32, 4 * 80);
#pragma unroll
      for (int vb = 0; vb < 2; ++vb) bf[vb] = tr_read2(pb + ks * 32 * 48 + vb * 32, 4 * 48);
#pragma unroll
      for (int fb = 0; fb < 3; ++fb)
#pragma unroll
        for (int vb = 0; vb < 2; ++vb) kv[fb][vb] = mfma(af[fb], bf[vb], kv[fb][vb]);
    }
    const int head = 8 * (cs - 11) + w;
    const unsigned rec = KVP_OFF + (head * 16 + rt) * KVP_REC;
    const unsigned o4 = rec + tok * 64 + g * 16, o2 = g < 2 ? rec + tok * 32 + g * 16 : 0x80000000u;
    const unsigned t5 = tok < 5 ? 0u : 0x80000000u;
    st16(rws, o4, __builtin_bit_cast(v4u, kv[0][0]));
    st16(rws, o4 + 1024, __builtin_bit_cast(v4u, kv[1][0]));
    st16(rws, o2 + 2048, __builtin_bit_cast(v4u, kv[2][0]));
    st16(rws, (o4 | t5) + 2560, __builtin_bit_cast(v4u, kv[0][1]));
    st16(rws, (o4 | t5) + 2880, __builtin_bit_cast(v4u, kv[1][1]));
    st16(rws, (o2 | t5) + 3200, __builtin_bit_cast(v4u, kv[2][1]));
  }
  STAMP_AT(0, 3);
  STAMP_FLUSH(0);
}

constexpr unsigned L2_RED = 0, L2_REDO = 61440, L2_KVS = L2_REDO + 81920, L2_FLAG = L2_KVS + 21 * 80 + 16, K2_LDS = L2_FLAG + 64;
#define OLD_AUX 16
#define OUT_AUX 16
#define K2_P 2

struct Stage { v4u a[5], b[4]; };
DI void k2_load(Stage& st, __amdgpu_buffer_rsrc_t ra, __amdgpu_buffer_rsrc_t rw, const unsigned (&vb)[4], unsigned vo, int ks) {
#pragma unroll
  for (int i = 0; i < 4; ++i) st.b[i] = __builtin_amdgcn_raw_buffer_load_b128(ra, vb[i], ks * 4096, 0);
#pragma unroll
  for (int nb = 0; nb < 5; ++nb) st.a[nb] = __builtin_amdgcn_raw_buffer_load_b128(rw, vo + nb * 1024, ks * 5120, 0);
}

template <int NH>
DI void k2_gemm(f32x4 (&acc)[5][4], __amdgpu_buffer_rsrc_t ra, __amdgpu_buffer_rsrc_t rw, const unsigned (&vb)[4], unsigned vo, int h0) {
  Stage st[K2_P];
#pragma unroll
  for (int i = 0; i < K2_P; ++i) k2_load(st[i], ra, rw, vb, vo, h0 + i);
  SB0();
#pragma unroll
  for (int i = 0; i < NH; ++i) {
    Stage& cur = st[i % K2_P];
#pragma unroll
    for (int nb = 0; nb < 5; ++nb)
#pragma unroll
      for (int m = 0; m < 4; ++m) acc[nb][m] = mfma(cur.a[nb], cur.b[m], acc[nb][m]);
    if (i + K2_P < NH) k2_load(cur, ra, rw, vb, vo, h0 + i + K2_P);
    SB0();
  }
}

__global__ __launch_bounds__(512, 2) void k2(K2Args p) {
  extern __shared__ __attribute__((aligned(16))) char smem[];
  LAS char* L = (LAS char*)smem;
  const int tid = threadIdx.x, lane = tid & 63, w = rfl(tid >> 6);
  const int bid = blockIdx.x, xcd = bid & 7, idx = bid >> 3;
  const int g = lane >> 4, tok = lane & 15;
  char* ws = p.ws;
  STAMP_DECL;
  STAMP_AT(1, 0);
  const int rt64 = (xcd >> 1) * 8 + (idx & 7), ct = (xcd & 1) * 4 + (idx >> 3);
  const __amdgpu_buffer_rsrc_t ra = mk_rsrc(ws + ACT_OFF + rt64 * (NKS * 4096), NKS * 4096);
  const __amdgpu_buffer_rsrc_t rw = mk_rsrc(ws + WT_OFF + ct * (NKS * 5120), NKS * 5120);
  const f32x4 z4 = {0.f, 0.f, 0.f, 0.f};
  f32x4 acc[5][4];
#pragma unroll
  for (int nb = 0; nb < 5; ++nb)
#pragma unroll
    for (int m = 0; m < 4; ++m) acc[nb][m] = z4;

  if (w >= 4) {
#define EARLYBAR
#define OPRIO 2
    __builtin_amdgcn_s_setprio(OPRIO);
    if (tid == 256) *(volatile LAS unsigned*)(L + L2_FLAG) = 0u;
    asm volatile("s_waitcnt lgkmcnt(0)\n\ts_barrier" ::: "memory");
    const __amdgpu_buffer_rsrc_t rws = mk_rsrc(ws, WS_BYTES);
    volatile LAS unsigned* flag = (volatile LAS unsigned*)(L + L2_FLAG);
    const int head = xcd * 4 + (idx & 3), rg = idx >> 2, ww = w - 4, t = tid - 256;
    v4u qf[4];
    const v4u wa0 = __builtin_amdgcn_raw_buffer_load_b128(rws, lane * 16, WPF_OFF + head * 2048, 0);
    const v4u wa1 = __builtin_amdgcn_raw_buffer_load_b128(rws, lane * 16 + 1024, WPF_OFF + head * 2048, 0);
#pragma unroll
    for (int m = 0; m < 4; ++m)
      qf[m] = __builtin_amdgcn_raw_buffer_load_b128(rws, lane * 16 + m * 1024, Q_OFF + (head * 128 + rg * 16 + ww * 4) * 1024, 0);
    f32x4 v[16];
#pragma unroll
    for (int r = 0; r < 16; ++r)
      v[r] = __builtin_bit_cast(f32x4, __builtin_amdgcn_raw_buffer_load_b128(rws, t * 16, KVP_OFF + (head * 16 + r) * KVP_REC, 0));
    SB0();
    STAMP_AT(1, 5);
    v4u fp[4], fn[4];
#pragma unroll
    for (int m = 0; m < 4; ++m) {
      const f32x4 p0 = mfma(wa0, qf[m], z4);
      f32x4 p1 = mfma(wa1, qf[m], z4);
      if (g != 0) p1 = p0;
      float mx = fmaxf(fmaxf(fmaxf(p0[0], p0[1]), fmaxf(p0[2], p0[3])), fmaxf(fmaxf(p1[0], p1[1]), fmaxf(p1[2], p1[3])));
      float mn = fminf(fminf(fminf(p0[0], p0[1]), fminf(p0[2], p0[3])), fminf(fminf(p1[0], p1[1]), fminf(p1[2], p1[3])));
      const float l2e = 1.4426950408889634f;
      const float mxs = red4_max(mx) * -l2e, mns = red4_max(-mn) * -l2e;
      const float keep = g == 0 ? 1.0f : 0.0f;
      f32x2 ep[4], en[4];
#pragma unroll
      for (int i = 0; i < 4; ++i) {
        const f32x4 s = i < 2 ? p0 : p1;
        const f32x2 x = f32x2{s[2 * (i & 1)], s[2 * (i & 1) + 1]};
        ep[i] = exp2v(x * l2e + mxs);
        en[i] = exp2v(x * -l2e + mns);
      }
      const f32x2 sp = (ep[0] + ep[1]) + (ep[2] + ep[3]) * keep, sn = (en[0] + en[1]) + (en[2] + en[3]) * keep;
      const float rp = __builtin_amdgcn_rcpf(red4_sum(sp[0] + sp[1])), rn = __builtin_amdgcn_rcpf(red4_sum(sn[0] + sn[1]));
      const float rpk = rp * keep, rnk = rn * keep;
      fp[m] = v4u{cvtpk(ep[0] * rp), cvtpk(ep[1] * rp), cvtpk(ep[2] * rpk), cvtpk(ep[3] * rpk)};
      fn[m] = v4u{cvtpk(en[0] * rn), cvtpk(en[1] * rn), cvtpk(en[2] * rnk), cvtpk(en[3] * rnk)};
    }
    SB0();
    STAMP_AT(1, 6);
    {
      f32x4 s = v[0];
#pragma unroll
      for (int r = 1; r < 16; ++r) s += v[r];
      int d, f0;
      if (t < 128) { d = (t & 63) >> 2; f0 = 16 * (t >> 6) + 4 * (t & 3); }
      else if (t < 160) { d = (t - 128) >> 1; f0 = 32 + 4 * (t & 1); }
      else if (t < 200) { const int u = t < 180 ? t - 160 : t - 180; d = 16 + (u >> 2); f0 = (t < 180 ? 0 : 16) + 4 * (u & 3); }
      else { d = 16 + ((t - 200) >> 1); f0 = 32 + 4 * (t & 1); }
      if (t < 210) *(LAS v2u*)(L + L2_KVS + d * 80 + f0 * 2) = cvt4(s);
    }
    asm volatile("s_waitcnt lgkmcnt(0)" ::: "memory");
    if (lane == 0) __hip_atomic_fetch_add((LAS unsigned*)(L + L2_FLAG), 1u, __ATOMIC_RELAXED, __HIP_MEMORY_SCOPE_WORKGROUP);
    while (flag[0] < 4u) __builtin_amdgcn_s_sleep(1);
    asm volatile("" ::: "memory");
    STAMP_AT(1, 1);
    {
      v4u ka[2][2];
#pragma unroll
      for (int b = 0; b < 2; ++b)
#pragma unroll
        for (int st = 0; st < 2; ++st) {
          const int d = b == 0 ? tok : (tok < 4 ? 16 + tok : 20);
          LAS char* src = L + L2_KVS + d * 80 + st * 40;
          const v2u lo = *(LAS v2u*)(src + g * 8), hi = *(LAS v2u*)(src + 32);
          ka[b][st] = v4u{lo.x, lo.y, hi.x, hi.y};
        }
      const int rto = rg * 4 + ww;
      const int Q0 = 5 * head + g, Q1 = 5 * head + 4;
      const unsigned o0 = (unsigned)(ACT_OFF + (rto * NKS + NKH + (Q0 >> 3)) * 4096 + (Q0 & 7) * 256 + tok * 16);
      const unsigned o1 = g == 0 ? (unsigned)(ACT_OFF + (rto * NKS + NKH + (Q1 >> 3)) * 4096 + (Q1 & 7) * 256 + tok * 16) : 0x80000000u;
      v2u a[4], b[4];
#pragma unroll
      for (int m = 0; m < 4; ++m) {
        f32x4 n0 = mfma(ka[0][0], fp[m], z4), n1 = mfma(ka[1][0], fp[m], z4);
        n0 = mfma(ka[0][1], fn[m], n0);
        n1 = mfma(ka[1][1], fn[m], n1);
        auto r = __builtin_amdgcn_permlane16_swap(f2u(n1[0]), f2u(n1[0]), false, false);
        const unsigned r1 = r[1];
        const float sc = __builtin_amdgcn_rcpf(u2f(r1) + 1e-6f);
        a[m] = cvt4(n0 * sc); b[m] = cvt4(n1 * sc);
        a[m].x = a[m].x == SENT ? 0x7fc07fc0u : a[m].x; a[m].y = a[m].y == SENT ? 0x7fc07fc0u : a[m].y;
        b[m].x = b[m].x == SENT ? 0x7fc07fc0u : b[m].x; b[m].y = b[m].y == SENT ? 0x7fc07fc0u : b[m].y;
        if (m & 1) {
          st16(rws, o0 + (m >> 1) * 2048, v4u{a[m - 1].x, a[m - 1].y, a[m].x, a[m].y});
          st16(rws, o1 + (m >> 1) * 2048, v4u{b[m - 1].x, b[m - 1].y, b[m].x, b[m].y});
        }
      }
    }
    STAMP_AT(1, 2);
    v4u wa[5][5];
#pragma unroll
    for (int i = 0; i < 5; ++i)
#pragma unroll
      for (int nb = 0; nb < 5; ++nb) wa[i][nb] = __builtin_amdgcn_raw_buffer_load_b128(rw, lane * 16 + nb * 1024, (NKH + 5 * ww + i) * 5120, 0);
    SB0();
    {
      const int qd = lane < 40 ? lane : 39;
      const unsigned co = (unsigned)((qd >> 3) * 4096 + 2048 + (qd & 7) * 256 + 252);
      for (unsigned spins = 0; spins < (1u << 16); ++spins) {
        const unsigned cv = __builtin_amdgcn_raw_buffer_load_b32(ra, co, (NKH + 5 * ww) * 4096, 16);
        if (!__any(cv == SENT)) break;
        __builtin_amdgcn_s_sleep(4);
      }
    }
    STAMP_AT(1, 7);
    v4u ob[5][4];
    for (unsigned spins = 0;; ++spins) {
#pragma unroll
      for (int i = 0; i < 5; ++i)
#pragma unroll
        for (int m = 0; m < 4; ++m) ob[i][m] = __builtin_amdgcn_raw_buffer_load_b128(ra, lane * 16 + m * 1024, (NKH + 5 * ww + i) * 4096, OLD_AUX);
      bool bad = false;
#pragma unroll
      for (int i = 0; i < 5; ++i)
#pragma unroll
        for (int m = 0; m < 4; ++m)
          bad |= (ob[i][m].x == SENT) | (ob[i][m].y == SENT) | (ob[i][m].z == SENT) | (ob[i][m].w == SENT);
      if (!__any(bad) || spins > (1u << 12)) break;
      __builtin_amdgcn_s_sleep(8);
    }
    STAMP_AT(1, 3);
#pragma unroll
    for (int i = 0; i < 5; ++i) {
      v4u fr[4];
#pragma unroll
      for (int pr = 0; pr < 2; ++pr) {
        fr[2 * pr] = v4u{ob[i][2 * pr].x, ob[i][2 * pr].y, ob[i][2 * pr + 1].x, ob[i][2 * pr + 1].y};
        fr[2 * pr + 1] = v4u{ob[i][2 * pr].z, ob[i][2 * pr].w, ob[i][2 * pr + 1].z, ob[i][2 * pr + 1].w};
      }
#pragma unroll
      for (int nb = 0; nb < 5; ++nb)
#pragma unroll
        for (int m = 0; m < 4; ++m) acc[nb][m] = mfma(wa[i][nb], fr[m], acc[nb][m]);
    }
#pragma unroll
    for (int m = 0; m < 4; ++m)
#pragma unroll
      for (int nb = 0; nb < 5; ++nb) *(LAS f32x4*)(L + L2_REDO + ((m * 4 + ww) * 5 + nb) * 1024 + lane * 16) = acc[nb][m];
    STAMP_AT(1, 4);
    __syncthreads();
    STAMP_FLUSH(1);
    return;
  }

  unsigned vb[4];
#pragma unroll
  for (int i = 0; i < 4; ++i) vb[i] = lane * 16 + ((w + i) & 3) * 1024;
  asm volatile("s_barrier" ::: "memory");
  STAMP_AT(1, 5);
  const float gsc = *((const float*)(ws + G_OFF) + rt64 * 64 + w * 16 + tok);
  if (w < 2) k2_gemm<14>(acc, ra, rw, vb, lane * 16, 14 * w);
  else k2_gemm<13>(acc, ra, rw, vb, lane * 16, 28 + 13 * (w - 2));
  STAMP_AT(1, 1);
#pragma unroll
  for (int i = 1; i < 4; ++i) {
    LAS char* dst = L + L2_RED + (((w + i) & 3) * 3 + (i - 1)) * 5120 + lane * 16;
#pragma unroll
    for (int nb = 0; nb < 5; ++nb) *(LAS f32x4*)(dst + nb * 1024) = acc[nb][i];
  }
  __syncthreads();
  STAMP_AT(1, 2);
  f32x4 r[5];
#pragma unroll
  for (int nb = 0; nb < 5; ++nb) r[nb] = acc[nb][0];
#pragma unroll
  for (int sl = 0; sl < 3; ++sl)
#pragma unroll
    for (int nb = 0; nb < 5; ++nb) r[nb] += *(LAS f32x4*)(L + L2_RED + ((w * 3 + sl) * 5 + nb) * 1024 + lane * 16);
  f32x4 ro[5];
#pragma unroll
  for (int nb = 0; nb < 5; ++nb) ro[nb] = *(LAS f32x4*)(L + L2_REDO + ((w * 4) * 5 + nb) * 1024 + lane * 16);
#pragma unroll
  for (int sl = 1; sl < 4; ++sl)
#pragma unroll
    for (int nb = 0; nb < 5; ++nb) ro[nb] += *(LAS f32x4*)(L + L2_REDO + ((w * 4 + sl) * 5 + nb) * 1024 + lane * 16);
#pragma unroll
  for (int nb = 0; nb < 5; ++nb) r[nb] = r[nb] * (1.0f - gsc) + ro[nb] * gsc;
  const __amdgpu_buffer_rsrc_t ro_ = mk_rsrc(p.out, S_ * D_ * 2);
  const unsigned op = (unsigned)(((rt64 * 64 + w * 16 + tok) * D_ + ct * 80) * 2);
  __builtin_amdgcn_raw_buffer_store_b128(cvt8(r[0], r[1]), ro_, op + 16 * g, 0, OUT_AUX);
  __builtin_amdgcn_raw_buffer_store_b128(cvt8(r[2], r[3]), ro_, op + 64 + 16 * g, 0, OUT_AUX);
  __builtin_amdgcn_raw_buffer_store_b64(cvt4(r[4]), ro_, op + 128 + 8 * g, 0, OUT_AUX);
  STAMP_AT(1, 3);
  STAMP_FLUSH(1);
}

extern "C" void kernel_launch(void* const* d_in, const int* in_sizes, int n_in, void* d_out, int out_size, void* d_ws,
                              size_t ws_size, hipStream_t stream) {
  K1Args a;
  a.x = (const u16*)d_in[0]; a.wqkv = (const u16*)d_in[1]; a.wphi = (const u16*)d_in[2]; a.wo = (const u16*)d_in[3];
  a.wg = (const u16*)d_in[5]; a.wu = (const u16*)d_in[6]; a.wd = (const u16*)d_in[7]; a.gw = (const u16*)d_in[8];
  a.ws = (char*)d_ws;
  K2Args b;
  b.ws = a.ws; b.out = (u16*)d_out;
  (void)hipFuncSetAttribute((const void*)k1, hipFuncAttributeMaxDynamicSharedMemorySize, K1_LDS);
  (void)hipFuncSetAttribute((const void*)k2, hipFuncAttributeMaxDynamicSharedMemorySize, K2_LDS);
  k1<<<256, 768, K1_LDS, stream>>>(a);
  k2<<<256, 512, K2_LDS, stream>>>(b);
}
```

```cpp
#include <hip/hip_runtime.h>

typedef unsigned short u16;
typedef unsigned v4u __attribute__((ext_vector_type(4)));
typedef unsigned v2u __attribute__((ext_vector_type(2)));
typedef float f32x4 __attribute__((ext_vector_type(4)));
typedef float f32x2 __attribute__((ext_vector_type(2)));
typedef short s16x4 __attribute__((ext_vector_type(4)));
typedef __bf16 bf16x8 __attribute__((ext_vector_type(8)));
typedef __bf16 bf16x2 __attribute__((ext_vector_type(2)));
#define LAS __attribute__((address_space(3)))
#define GAS __attribute__((address_space(1)))
#define DI __device__ __forceinline__

constexpr int S_ = 2048, D_ = 640, F_ = 1728;
constexpr int NKS = 74, NKH = 54;
constexpr unsigned ACT_OFF = 0, ACT_BYTES = 32u * NKS * 4 * 1024;
constexpr unsigned WT_OFF = ACT_OFF + ACT_BYTES, WT_BYTES = 8u * NKS * 5 * 1024;
constexpr unsigned Q_OFF = WT_OFF + WT_BYTES, Q_BYTES = 32u * 128 * 1024;
constexpr unsigned KVP_REC = 3360;
constexpr unsigned KVP_OFF = Q_OFF + Q_BYTES, KVP_BYTES = 32u * 16 * KVP_REC;
constexpr unsigned G_OFF = KVP_OFF + KVP_BYTES, G_BYTES = S_ * 4;
constexpr unsigned WPF_OFF = G_OFF + G_BYTES, WPF_BYTES = 32u * 2 * 1024;
constexpr unsigned WS_BYTES = WPF_OFF + WPF_BYTES;
constexpr unsigned SENT = 0xFFFFFFFFu;

constexpr unsigned L_W = 0, WSLOT = 24576, L_X = 4 * WSLOT, XSLOT = 16384, L_WPHI = L_X + 2 * XSLOT, L_ZERO = L_WPHI + 6400,
                   L_DUMMY = L_WPHI + 8192, K1_LDS = 163840;
constexpr unsigned L_PK = 0, L_V = 8 * 128 * 80;

DI int fresh_lane() {
  int l;
  asm volatile("v_mbcnt_lo_u32_b32 %0, -1, 0\n\tv_mbcnt_hi_u32_b32 %0, -1, %0" : "=v"(l));
  return l;
}
#define LANE_VARS(l) const int lane = (l), g = lane >> 4, q = (lane >> 2) & 3, pp = lane & 3, tok = lane & 15; (void)q; (void)pp; (void)tok; (void)g
DI unsigned rfl(unsigned x) { return __builtin_amdgcn_readfirstlane(x); }
DI unsigned cvtpk(f32x2 v) {
  bf16x2 b = __builtin_convertvector(v, bf16x2);
  return __builtin_bit_cast(unsigned, b);
}
DI unsigned cvtpk(float lo, float hi) { return cvtpk(f32x2{lo, hi}); }
DI v2u cvt4(f32x4 v) { return v2u{cvtpk(v[0], v[1]), cvtpk(v[2], v[3])}; }
DI v4u cvt8(f32x4 a, f32x4 b) { return v4u{cvtpk(a[0], a[1]), cvtpk(a[2], a[3]), cvtpk(b[0], b[1]), cvtpk(b[2], b[3])}; }
DI float bf2f(unsigned b) { return __builtin_bit_cast(float, b << 16); }
DI f32x4 mfma(v4u a, v4u b, f32x4 c) {
  return __builtin_amdgcn_mfma_f32_16x16x32_bf16(__builtin_bit_cast(bf16x8, a), __builtin_bit_cast(bf16x8, b), c, 0, 0, 0);
}
DI v2u tr_read(LAS char* p) {
  s16x4 r = __builtin_amdgcn_ds_read_tr16_b64_v4i16((LAS s16x4*)p);
  return __builtin_bit_cast(v2u, r);
}
DI v4u tr_read2(LAS char* p, int d) {
  v2u a = tr_read(p), b = tr_read(p + d);
  return v4u{a.x, a.y, b.x, b.y};
}
DI __amdgpu_buffer_rsrc_t mk_rsrc(const void* p, unsigned bytes) {
  return __builtin_amdgcn_make_buffer_rsrc((void*)p, (short)0, (int)bytes, 0x00020000);
}
DI float u2f(unsigned u) { return __builtin_bit_cast(float, u); }
DI unsigned f2u(float f) { return __builtin_bit_cast(unsigned, f); }
DI float red4_max(float x) {
  auto r = __builtin_amdgcn_permlane16_swap(f2u(x), f2u(x), false, false);
  unsigned r0 = r[0], r1 = r[1];
  x = fmaxf(u2f(r0), u2f(r1));
  auto t = __builtin_amdgcn_permlane32_swap(f2u(x), f2u(x), false, false);
  r0 = t[0]; r1 = t[1];
  return fmaxf(u2f(r0), u2f(r1));
}
DI float red4_sum(float x) {
  auto r = __builtin_amdgcn_permlane16_swap(f2u(x), f2u(x), false, false);
  unsigned r0 = r[0], r1 = r[1];
  x = u2f(r0) + u2f(r1);
  auto t = __builtin_amdgcn_permlane32_swap(f2u(x), f2u(x), false, false);
  r0 = t[0]; r1 = t[1];
  return u2f(r0) + u2f(r1);
}
DI f32x2 exp2v(f32x2 a) { return f32x2{__builtin_amdgcn_exp2f(a[0]), __builtin_amdgcn_exp2f(a[1])}; }
DI void st16(__amdgpu_buffer_rsrc_t r, unsigned off, v4u v) { __builtin_amdgcn_raw_buffer_store_b128(v, r, off, 0, 16); }
DI void st8(__amdgpu_buffer_rsrc_t r, unsigned off, v2u v) { __builtin_amdgcn_raw_buffer_store_b64(v, r, off, 0, 16); }
#define AUX_DELAY 6
#define PF_MODE 1
#define PF_D0 8
#define PF_D1 1
#define SB0() __builtin_amdgcn_sched_barrier(0)
#define SYNC(n) do { SB0(); asm volatile("s_waitcnt vmcnt(" #n ") lgkmcnt(0)\n\ts_barrier" ::: "memory"); SB0(); } while (0)
#define NT_A "nt"
#define LDSDMA(m0v, vo, rs, so) \
  asm volatile("s_mov_b32 m0, %0\n\ts_nop 0\n\tbuffer_load_dwordx4 %1, %2, %3 offen " NT_A " lds" :: "s"(m0v), "v"(vo), "s"(rs), "s"(so) : "memory")

#define STAMP_DECL
#define STAMP_AT(kern, i)
#define STAMP_FLUSH(kern)
#define STAMP_ARG
#define STAMP_PASS

struct K1Args {
  const u16 *x, *wqkv, *wphi, *wo, *wg, *wu, *wd, *gw;
  char* ws;
};
struct K2Args {
  char* ws;
  u16* out;
};

#define SYNCL() do { SB0(); asm volatile("s_waitcnt lgkmcnt(0)\n\ts_barrier" ::: "memory"); SB0(); } while (0)
#define NT_D ""
#define NT_P ""
#define DMA(m0v, vo, rs, so, off) \
  asm volatile("s_mov_b32 m0, %0\n\ts_nop 0\n\tbuffer_load_dwordx4 %1, %2, %3 offen offset:%4 " NT_D " lds" :: "s"(m0v), "v"(vo), "s"(rs), "s"(so), "i"(off) : "memory")
#define DMAP(m0v, vo, rs, so, off) \
  asm volatile("s_mov_b32 m0, %0\n\ts_nop 0\n\tbuffer_load_dwordx4 %1, %2, %3 offen offset:%4 " NT_P " lds" :: "s"(m0v), "v"(vo), "s"(rs), "s"(so), "i"(off) : "memory")

template <int MODE> struct MI { static constexpr int NB = MODE == 1 ? 5 : 6; };
template <int MODE, int B, int R, int SLOT> DI v2u a_read(LAS char* L, unsigned (&aoff)[5], LAS char* gp) {
  if constexpr (MODE == 2 && B == 5) return *(LAS v2u*)(gp + R * 8);
  else {
    constexpr int I = MODE == 0 ? B >> 1 : B;
    const v2u t = tr_read(L + L_W + (SLOT & 1) * WSLOT + aoff[I] + ((MODE == 0 && (B & 1)) ? 12288 : 0) + R * 1536);
    if constexpr (R == 1 && (MODE != 0 || (B & 1))) {
      if constexpr (SLOT == 1) asm volatile("v_add_u32 %0, 0xc000, %0" : "+v"(aoff[I]));
      if constexpr (SLOT == 3) asm volatile("v_subrev_u32 %0, 0xc000, %0" : "+v"(aoff[I]));
    }
    return t;
  }
}
DI void mfma_ip(f32x4& c, const v4u& a, const v4u& b) {
  asm volatile("v_mfma_f32_16x16x32_bf16 %0, %1, %2, %0" : "+v"(c) : "v"(a), "v"(b));
}
template <int MODE, bool NEXT, int T, int J = 0>
DI void k1_step(f32x4 (&acc)[4][MI<MODE>::NB], v4u (&a)[4], v4u (&x)[2][4], LAS char* L, unsigned (&aoff)[5],
                const unsigned (&xoff)[2], LAS char* gp) {
  constexpr int NB = MI<MODE>::NB, R0 = T * NB;
  if constexpr (J < 4 * NB) {
    constexpr int b = J >> 2, r = J & 3, nb = b + 2;
    if constexpr (r < 2) {
      if constexpr (nb < NB || NEXT) {
        v2u t = a_read<MODE, nb % NB, r, (nb < NB ? T : T + 1) & 3>(L, aoff, gp + T * 64);
        if constexpr (r == 0) { a[(R0 + nb) & 3].x = t.x; a[(R0 + nb) & 3].y = t.y; }
        else { a[(R0 + nb) & 3].z = t.x; a[(R0 + nb) & 3].w = t.y; }
      }
    } else if constexpr (r == 2 && b < 4 && NEXT)
      x[(T + 1) & 1][b] = *(LAS v4u*)(L + xoff[(T + 1) & 1] + ((((T + 1) >> 1) & 1) * XSLOT + b * 2048));
    mfma_ip(acc[r][b], a[(R0 + b) & 3], x[T & 1][r]);
    SB0();
    k1_step<MODE, NEXT, T, J + 1>(acc, a, x, L, aoff, xoff, gp);
  }
}
template <int MODE>
DI void k1_mainloop(f32x4 (&acc)[4][MI<MODE>::NB], LAS char* L, unsigned (&aoff)[5], unsigned xoff0, int g STAMP_ARG) {
  unsigned xoff[2] = {xoff0 + L_X, (xoff0 ^ 64) + L_X};
  asm volatile("" : "+v"(xoff[0]), "+v"(xoff[1]));
  v4u a[4], x[2][4];
  unsigned gpo = L_WPHI + g * 16;
  asm volatile("" : "+v"(gpo));
  LAS char* gp = L + gpo;
#pragma unroll
  for (int m = 0; m < 4; ++m)
#pragma unroll
    for (int b = 0; b < MI<MODE>::NB; ++b) acc[m][b] = f32x4{0.f, 0.f, 0.f, 0.f};
  SYNCL();
  STAMP_AT(0, 4);
#pragma unroll
  for (int m = 0; m < 4; ++m) x[0][m] = *(LAS v4u*)(L + xoff[0] + m * 2048);
  {
    const v2u t0 = a_read<MODE, 0, 0, 0>(L, aoff, gp), t1 = a_read<MODE, 0, 1, 0>(L, aoff, gp);
    const v2u t2 = a_read<MODE, 1, 0, 0>(L, aoff, gp), t3 = a_read<MODE, 1, 1, 0>(L, aoff, gp);
    a[0] = v4u{t0.x, t0.y, t1.x, t1.y}; a[1] = v4u{t2.x, t2.y, t3.x, t3.y};
  }
#pragma unroll 1
  for (int i = 0; i < 16; i += 4) {
    SYNCL(); k1_step<MODE, true, 0>(acc, a, x, L, aoff, xoff, gp);
    SYNCL(); k1_step<MODE, true, 1>(acc, a, x, L, aoff, xoff, gp);
    SYNCL(); k1_step<MODE, true, 2>(acc, a, x, L, aoff, xoff, gp);
    SYNCL(); k1_step<MODE, true, 3>(acc, a, x, L, aoff, xoff, gp);
    gp += 256;
  }
  SYNCL(); k1_step<MODE, true, 0>(acc, a, x, L, aoff, xoff, gp);
  SYNCL(); k1_step<MODE, true, 1>(acc, a, x, L, aoff, xoff, gp);
  SYNCL(); k1_step<MODE, true, 2>(acc, a, x, L, aoff, xoff, gp);
  k1_step<MODE, false, 3>(acc, a, x, L, aoff, xoff, gp);
  asm volatile("s_nop 15" ::: "memory");
  SB0();
}

DI unsigned gelu_mul2(f32x2 x, f32x2 u) {
  const float k0 = -2.0f * 0.7978845608028654f * 1.4426950408889634f, k1 = k0 * 0.044715f;
  f32x2 t = x * x;
  f32x2 a = x * (t * k1 + k0);
  f32x2 d = exp2v(a) + 1.0f;
  f32x2 r = f32x2{__builtin_amdgcn_rcpf(d[0]), __builtin_amdgcn_rcpf(d[1])};
  return cvtpk((x * u) * r);
}
DI v2u gelu_mul4(f32x4 x, f32x4 u) {
  return v2u{gelu_mul2(f32x2{x[0], x[1]}, f32x2{u[0], u[1]}), gelu_mul2(f32x2{x[2], x[3]}, f32x2{u[2], u[3]})};
}

__global__ __launch_bounds__(768, 3) void k1(K1Args p) {
  extern __shared__ __attribute__((aligned(16))) char smem[];
  LAS char* L = (LAS char*)smem;
  const int tid = threadIdx.x, lane = tid & 63, w = rfl(tid >> 6);
  const int bid = blockIdx.x, xcd = bid & 7, idx = bid >> 3;
  const unsigned long long CSMAP = 0xDC87BA65E943F210ull;
  const int rt = (xcd & 1) * 8 + (idx & 7), cs = (int)(CSMAP >> (4 * ((xcd >> 1) * 4 + (idx >> 3)))) & 15;
  const int g = lane >> 4, q = (lane >> 2) & 3, pp = lane & 3, tok = lane & 15;
  char* ws = p.ws;
  const __amdgpu_buffer_rsrc_t rws = mk_rsrc(ws, WS_BYTES);
  STAMP_DECL;
  STAMP_AT(0, 0);

  if (cs == 15) {
    if (w >= 8) {
      const int head = 2 * rt + ((w >> 1) & 1), f = 16 * (w & 1) + tok;
      const u16* wp = p.wphi + head * 400 + (f < 20 ? f : 0);
      unsigned e[5];
#pragma unroll
      for (int j = 0; j < 4; ++j) e[j] = wp[(4 * g + j) * 20];
      e[4] = wp[(16 + g) * 20];
      v4u fr = v4u{e[0] | e[1] << 16, e[2] | e[3] << 16, e[4], 0u};
      if (f >= 20) fr = v4u{0u, 0u, 0u, 0u};
      st16(rws, WPF_OFF + (head * 2 + (w & 1)) * 1024 + lane * 16, fr);
      return;
    }
    const __amdgpu_buffer_rsrc_t rd = mk_rsrc(p.wd, F_ * D_ * 2), ro = mk_rsrc(p.wo, D_ * D_ * 2);
    unsigned vlo[5];
#pragma unroll
    for (int j = 0; j < 5; ++j) {
      const int Lc = ((w & 3) * 5 + j) * 64 + lane;
      vlo[j] = (unsigned)((Lc / 80) * 1280 + (Lc % 80) * 16);
    }
    auto issue = [&](int sl) {
      const int s = rt + 16 * sl;
      int b0, b1;
      if (s < NKH) {
        const int u = s / 6, e = s % 6;
        if (e < 4) { b0 = 192 * u + 48 * e; b1 = b0 + 16; } else { b0 = 192 * u + 96 * (e - 4) + 32; b1 = b0 + 48; }
      } else { b0 = 32 * (s - NKH); b1 = b0 + 16; }
      const __amdgpu_buffer_rsrc_t rs = s < NKH ? rd : ro;
      const unsigned so = (unsigned)((w < 4 ? b0 : b1) * 1280), m0 = (unsigned)((sl & 3) * 40960 + w * 5120);
#pragma unroll
      for (int j = 0; j < 5; ++j) LDSDMA(m0 + j * 1024, vlo[j], rs, so);
    };
    auto retile = [&](int sl) {
      const int s = rt + 16 * sl;
      LAS char* base = L + (sl & 3) * 40960 + (4 * g + q) * 1280 + w * 160;
      const unsigned dst = s < NKS ? WT_OFF + (w * NKS + s) * 5120 + lane * 16 : 0x80000000u;
#pragma unroll
      for (int nb = 0; nb < 5; ++nb) {
        const int col = nb < 4 ? 32 * (nb >> 1) + 8 * pp + 4 * (nb & 1) : 64 + 4 * pp;
        st16(rws, dst + nb * 1024, tr_read2(base + col * 2, 16 * 1280));
      }
    };
    for (int i = 0; i < AUX_DELAY; ++i) __builtin_amdgcn_s_sleep(16);
    issue(0); issue(1); issue(2); issue(3);
    SYNC(15); retile(0);
    SYNC(15); issue(4); retile(1);
    SYNC(20); retile(2);
    STAMP_AT(0, 1);
    SYNC(20); retile(3);
    SYNC(15); retile(4);
    STAMP_AT(0, 2);
    STAMP_FLUSH(0);
    return;
  }

  const bool isGU = cs < 9, isKV = cs >= 11;
  const __amdgpu_buffer_rsrc_t rx = mk_rsrc(p.x + rt * 128 * D_, (unsigned)((S_ - rt * 128) * D_ * 2));
  const unsigned pitch = isGU ? F_ * 2 : 3 * D_ * 2, total = D_ * pitch;
  unsigned boffA, boffB;
  if (isGU) boffA = boffB = 384u * cs;
  else if (!isKV) { boffA = 640u * (cs - 9); boffB = boffA + 256u; }
  else { const int j = cs - 11; boffA = 1280u + 320u * j - ((j & 1) ? 64u : 0u); boffB = boffA + 1280u; }
  const char* baseA = (const char*)(isGU ? p.wg : p.wqkv) + boffA;
  const char* baseB = (const char*)(isGU ? p.wu : p.wqkv) + boffB;
  if (w >= 8) {
    __builtin_amdgcn_s_setprio(3);
    const int lw = w - 8, sub = lw >> 1;
    const int r0 = 32 * lw + (lane >> 3);
    const unsigned vx0 = (unsigned)(r0 * 1280 + (((lane & 7) ^ ((r0 >> 1) & 7)) * 16)), vx1 = (vx0 ^ 64u) + 8 * 1280 - 1024;
    const unsigned m0x = L_X + lw * 4096;
#define LOAD_X(stage, slot, e) do { const unsigned m0_ = m0x + (unsigned)(slot) * XSLOT + (e) * 2048, so_ = (unsigned)(stage) * 128 + (e) * 16 * 1280; \
      DMA(m0_, vx0, rx, so_, 0); DMA(m0_, vx1, rx, so_, 1024); } while (0)
    if (cs == 9 && lw < 2) {
      const __amdgpu_buffer_rsrc_t rg = mk_rsrc(p.gw, D_ * 2);
      DMA(L_WPHI + lw * 1024, (unsigned)(lw * 1024 + lane * 16), rg, 0, 0);
    }
    LOAD_X(0, 0, 0); LOAD_X(0, 0, 1);
    const __amdgpu_buffer_rsrc_t rw = mk_rsrc((sub ? baseB : baseA) + (lw & 1) * 16 * pitch, total - (sub ? boffB : boffA) - (lw & 1) * 16 * pitch);
    unsigned vw[6];
#pragma unroll
    for (int j = 0; j < 3; ++j) {
      const int Lc = 64 * j + lane, kr = Lc / 24, pos = Lc % 24;
      const unsigned c0 = (unsigned)((pos >> 3) * 128 + (((pos & 7) ^ (kr & 2)) * 16));
      vw[j] = (unsigned)kr * pitch + c0 - 1024u * j;
      vw[3 + j] = (unsigned)(kr + 8) * pitch + (c0 ^ 64u) - 1024u * j;
    }
    const unsigned m0w = L_W + sub * 12288 + (lw & 1) * 6144, wstep = 32 * pitch;
#define LOAD_W(s) do { const unsigned m0_ = m0w + (unsigned)((s) & 3) * WSLOT, so_ = (unsigned)(s) * wstep; \
      DMA(m0_, vw[0], rw, so_, 0); DMA(m0_, vw[1], rw, so_, 1024); DMA(m0_, vw[2], rw, so_, 2048); \
      DMA(m0_ + 3072, vw[3], rw, so_, 0); DMA(m0_ + 3072, vw[4], rw, so_, 1024); DMA(m0_ + 3072, vw[5], rw, so_, 2048); } while (0)
    const __amdgpu_buffer_rsrc_t rpf = lw == 3 ? rx : mk_rsrc(((rt & 1) ? baseB : baseA) + 8 * (rt >> 1) * pitch, total - ((rt & 1) ? boffB : boffA) - 8 * (rt >> 1) * pitch);
    const unsigned vpf = lw == 3 ? (unsigned)((8 * cs + (lane >> 3)) * 1280 + (lane & 7) * 16) : lw == 0 ? vw[0] : lw == 1 ? vw[1] + 1024u : vw[2] + 2048u;
    const unsigned pfstep = lw == 3 ? 128u : 64u * pitch;
#define PF(stage) DMAP(L_DUMMY + lw * 1024, vpf, rpf, (unsigned)(stage) * pfstep, 0)
#define PFN(a, b) SYNC(a)
#define LOAD_XX(stage) do { LOAD_X(stage, (stage) & 1, 0); LOAD_X(stage, (stage) & 1, 1); } while (0)
    LOAD_W(0);
    LOAD_W(1); LOAD_XX(1);
    LOAD_W(2); PF(2); PF(3);
    PFN(18, 16);
    PFN(8, 6); LOAD_W(3); PF(4);
    PFN(9, 6); LOAD_XX(2); LOAD_W(4); PF(5);
    PFN(12, 10); LOAD_W(5); PF(6);
    PFN(8, 6); LOAD_XX(3); LOAD_W(6); PF(7);
    PFN(12, 10); LOAD_W(7); PF(8);
    PFN(8, 6); LOAD_XX(4); LOAD_W(8); PF(9);
    PFN(12, 10); LOAD_W(9);
    PFN(7, 6); LOAD_XX(5); LOAD_W(10);
    SYNC(10); LOAD_W(11);
#pragma unroll 1
    for (int i = 9; i < 15; i += 2) {
      SYNC(6); LOAD_X((i + 3) >> 1, ((i + 3) >> 1) & 1, 0); LOAD_X((i + 3) >> 1, ((i + 3) >> 1) & 1, 1); LOAD_W(i + 3);
      SYNC(10); LOAD_W(i + 4);
    }
    SYNC(6); LOAD_X(9, 1, 0); LOAD_X(9, 1, 1); LOAD_W(18);
    SYNC(10); LOAD_W(19);
    if (isKV) {
      const __amdgpu_buffer_rsrc_t rp = mk_rsrc(p.wphi + 8 * (cs - 11) * 400, 6400);
      DMA(L_WPHI + lw * 2048, (unsigned)(lw * 2048 + lane * 16), rp, 0, 0);
      DMA(L_WPHI + lw * 2048, (unsigned)(lw * 2048 + lane * 16), rp, 0, 1024);
    }
    SYNC(6);
    SYNC(0);
    return;
  }
  const int mh = w & 1, n = w >> 1;
  const int rt64 = rt * 2 + mh;
#define FRAG_VARS LANE_VARS(fresh_lane()); const int sw = ((q >> 1) & 1) | ((g & 1) << 1); \
  const unsigned xoff0 = (unsigned)((64 * mh + tok) * 128 + ((g ^ (tok >> 1)) * 16))

  if (isGU) {
    FRAG_VARS;
    unsigned aoff[5];
#pragma unroll
    for (int b = 0; b < 3; ++b) {
      const int ch = 6 * n + 2 * b + (pp >> 1);
      aoff[b] = (unsigned)((8 * g + q) * 384 + (ch >> 3) * 128 + (((ch & 7) ^ (2 * sw)) * 16) + (pp & 1) * 8);
    }
    aoff[3] = aoff[4] = 0;
    f32x4 acc[4][6];
    k1_mainloop<0>(acc, L, aoff, xoff0, g STAMP_PASS);
    STAMP_AT(0, 1);
    {
    LANE_VARS(fresh_lane());
    const unsigned actA = ACT_OFF + ((rt64 * NKS + 6 * cs + n) * 4) * 1024 + lane * 16;
    const unsigned actB = ACT_OFF + ((rt64 * NKS + 6 * cs + 4 + (n >> 1)) * 4 + 2 * (n & 1)) * 1024 + lane * 16;
    v2u h2[4];
#pragma unroll
    for (int m = 0; m < 4; ++m) h2[m] = gelu_mul4(acc[m][4], acc[m][5]);
    const bool odd = n & 1;
    *(LAS v4u*)(L + L_WPHI + w * 1024 + lane * 16) = odd ? v4u{h2[0].x, h2[0].y, h2[1].x, h2[1].y} : v4u{h2[2].x, h2[2].y, h2[3].x, h2[3].y};
    const v2u k0 = odd ? h2[2] : h2[0], k1 = odd ? h2[3] : h2[1];
#pragma unroll
    for (int m = 0; m < 4; ++m) {
      const v2u h0 = gelu_mul4(acc[m][0], acc[m][1]), h1 = gelu_mul4(acc[m][2], acc[m][3]);
      st16(rws, actA + m * 1024, v4u{h0.x, h0.y, h1.x, h1.y});
      if (m == 1) {
        SYNCL();
        const v4u o = *(LAS v4u*)(L + L_WPHI + (w ^ 2) * 1024 + lane * 16);
        st16(rws, actB, odd ? v4u{o.x, o.y, k0.x, k0.y} : v4u{k0.x, k0.y, o.x, o.y});
        st16(rws, actB + 1024, odd ? v4u{o.z, o.w, k1.x, k1.y} : v4u{k1.x, k1.y, o.z, o.w});
      }
    }
    }
    STAMP_AT(0, 2);
    STAMP_FLUSH(0);
    return;
  }

#define QKV_AOFF \
    FRAG_VARS; \
    unsigned aoff[5]; \
    { \
      const int offch = isKV ? ((cs - 11) & 1) * 4 : (n >> 1) * 4; \
      _Pragma("unroll") for (int b = 0; b < 5; ++b) { \
        const int u = 2 * offch + 20 * (n & 1) + 5 * pp + b, ch = u >> 1; \
        aoff[b] = (unsigned)((n >> 1) * 12288 + (8 * g + q) * 384 + (ch >> 3) * 128 + (((ch & 7) ^ (2 * sw)) * 16) + (u & 1) * 8); \
      } \
    }
  v4u c01[4], c23[4];
  v2u c4[4];
  if (cs == 9) {
    QKV_AOFF
    f32x4 acc[4][6];
    k1_mainloop<2>(acc, L, aoff, xoff0, g STAMP_PASS);
#pragma unroll
    for (int m = 0; m < 4; ++m) { c01[m] = cvt8(acc[m][0], acc[m][1]); c23[m] = cvt8(acc[m][2], acc[m][3]); c4[m] = cvt4(acc[m][4]); }
    if (n == 0) {
      LANE_VARS(fresh_lane());
      const float a = g == 0 ? acc[0][5][0] : g == 1 ? acc[1][5][0] : g == 2 ? acc[2][5][0] : acc[3][5][0];
      __builtin_amdgcn_raw_buffer_store_b32(f2u(1.0f / (1.0f + __expf(-a))), rws, G_OFF + (rt64 * 64 + lane) * 4, 0, 16);
    }
  } else {
    QKV_AOFF
    f32x4 acc[4][5];
    k1_mainloop<1>(acc, L, aoff, xoff0, g STAMP_PASS);
#pragma unroll
    for (int m = 0; m < 4; ++m) { c01[m] = cvt8(acc[m][0], acc[m][1]); c23[m] = cvt8(acc[m][2], acc[m][3]); c4[m] = cvt4(acc[m][4]); }
  }
  STAMP_AT(0, 1);
  {
  LANE_VARS(fresh_lane());
  const int tid = w * 64 + lane;
  if (!isKV) {
    const int head = 16 * (cs - 9) + 4 * n + g;
    const unsigned qp = Q_OFF + (head * 128 + rt64 * 4) * 1024 + tok * 16;
#pragma unroll
    for (int m = 0; m < 4; ++m) {
      st16(rws, qp + m * 1024, v4u{c01[m].x, c01[m].y, c4[m].x & 0xffffu, 0u});
      st16(rws, qp + m * 1024 + 256, v4u{c01[m].z, c01[m].w, c4[m].x >> 16, 0u});
      st16(rws, qp + m * 1024 + 512, v4u{c23[m].x, c23[m].y, c4[m].y & 0xffffu, 0u});
      st16(rws, qp + m * 1024 + 768, v4u{c23[m].z, c23[m].w, c4[m].y >> 16, 0u});
    }
    const unsigned op = ACT_OFF + ((2 * rt + cs - 9) * NKS + NKH) * 4096 + tid * 16;
#pragma unroll
    for (int i = 0; i < 10; ++i) st16(rws, op + i * 8192, v4u{SENT, SENT, SENT, SENT});
    STAMP_AT(0, 2);
    STAMP_FLUSH(0);
    return;
  }

  const int hl = 4 * (n & 1) + g;
  LAS char* xch = L + L_DUMMY + (w & 3) * 5120;
  if (n < 2) {
#pragma unroll
    for (int mm = 0; mm < 2; ++mm) {
      *(LAS v4u*)(xch + mm * 2560 + lane * 16) = c01[2 + mm];
      *(LAS v4u*)(xch + mm * 2560 + 1024 + lane * 16) = c23[2 + mm];
      *(LAS v2u*)(xch + mm * 2560 + 2048 + lane * 8) = c4[2 + mm];
    }
  }
  v4u wf[5][3];
  {
    LAS char* wb = L + (pp == g ? L_WPHI + (unsigned)(hl * 800 + q * 40) : L_ZERO);
#pragma unroll
    for (int bo = 0; bo < 5; ++bo)
#pragma unroll
      for (int s3 = 0; s3 < 3; ++s3) {
        v2u lo = tr_read(wb + s3 * 320 + bo * 8), hi = v2u{0u, 0u};
        if (s3 < 2) hi = tr_read(wb + s3 * 320 + 160 + bo * 8);
        wf[bo][s3] = v4u{lo.x, lo.y, hi.x, hi.y};
      }
  }
  SYNCL();
  if (n >= 2) {
#pragma unroll
    for (int m = 0; m < 4; ++m) {
      LAS char* dst = L + L_V + (hl * 128 + 64 * mh + 16 * m + tok) * 48;
      *(LAS v4u*)(dst) = c01[m];
      *(LAS v4u*)(dst + 16) = c23[m];
      *(LAS v4u*)(dst + 32) = v4u{c4[m].x, c4[m].y, 0x00003f80u, 0u};
    }
#pragma unroll
    for (int mm = 0; mm < 2; ++mm) {
      c01[mm] = *(LAS v4u*)(xch + mm * 2560 + lane * 16);
      c23[mm] = *(LAS v4u*)(xch + mm * 2560 + 1024 + lane * 16);
      c4[mm] = *(LAS v2u*)(xch + mm * 2560 + 2048 + lane * 8);
    }
  }
  {
    LAS char* dst0 = L + L_PK + (hl * 128 + 64 * mh + (n >= 2 ? 32 : 0) + tok) * 80;
    f32x4 pk[2][5];
#pragma unroll
    for (int m = 0; m < 2; ++m) {
      const v4u kf[3] = {c01[m], c23[m], v4u{c4[m].x, c4[m].y, 0u, 0u}};
#pragma unroll
      for (int bo = 0; bo < 5; ++bo) {
        pk[m][bo] = f32x4{0.f, 0.f, 0.f, 0.f};
#pragma unroll
        for (int s3 = 0; s3 < 3; ++s3) pk[m][bo] = mfma(wf[bo][s3], kf[s3], pk[m][bo]);
      }
    }
#pragma unroll
    for (int m = 0; m < 2; ++m) {
      float mx = pk[m][0][0], mn = pk[m][0][0];
#pragma unroll
      for (int bo = 0; bo < 5; ++bo)
#pragma unroll
        for (int r = 0; r < 4; ++r) { mx = fmaxf(mx, pk[m][bo][r]); mn = fminf(mn, pk[m][bo][r]); }
      const float l2e = 1.4426950408889634f;
      const float mxs = -mx * l2e, mns = mn * l2e;
      f32x2 ep[10], en[10];
#pragma unroll
      for (int i = 0; i < 10; ++i) {
        const f32x2 v = f32x2{pk[m][i >> 1][2 * (i & 1)], pk[m][i >> 1][2 * (i & 1) + 1]};
        ep[i] = v * l2e + mxs;
        en[i] = v * -l2e + mns;
      }
#pragma unroll
      for (int i = 0; i < 10; ++i) { ep[i] = exp2v(ep[i]); en[i] = exp2v(en[i]); }
      const f32x2 sp = (((ep[0] + ep[1]) + (ep[2] + ep[3])) + ((ep[4] + ep[5]) + (ep[6] + ep[7]))) + (ep[8] + ep[9]);
      const f32x2 sn = (((en[0] + en[1]) + (en[2] + en[3])) + ((en[4] + en[5]) + (en[6] + en[7]))) + (en[8] + en[9]);
      const float rp = __builtin_amdgcn_rcpf(sp[0] + sp[1]), rn = __builtin_amdgcn_rcpf(sn[0] + sn[1]);
      unsigned o[20];
#pragma unroll
      for (int i = 0; i < 10; ++i) { o[i] = cvtpk(ep[i] * rp); o[10 + i] = cvtpk(en[i] * rn); }
      LAS char* dst = dst0 + m * 16 * 80;
#pragma unroll
      for (int i = 0; i < 5; ++i) *(LAS v4u*)(dst + i * 16) = v4u{o[4 * i], o[4 * i + 1], o[4 * i + 2], o[4 * i + 3]};
    }
  }
  STAMP_AT(0, 2);
  SYNCL();
  {
    f32x4 kv[3][2];
#pragma unroll
    for (int fb = 0; fb < 3; ++fb) { kv[fb][0] = f32x4{0.f, 0.f, 0.f, 0.f}; kv[fb][1] = f32x4{0.f, 0.f, 0.f, 0.f}; }
    LAS char* pa = L + L_PK + (w * 128 + 8 * g + q) * 80 + pp * 8;
    LAS char* pb = L + L_V + (w * 128 + 8 * g + q) * 48 + pp * 8;
#pragma unroll
    for (int ks = 0; ks < 4; ++ks) {
      v4u af[3], bf[2];
#pragma unroll
      for (int fb = 0; fb < 3; ++fb) af[fb] = tr_read2(pa + ks * 32 * 80 + fb * 32, 4 * 80);
#pragma unroll
      for (int vb = 0; vb < 2; ++vb) bf[vb] = tr_read2(pb + ks * 32 * 48 + vb * 32, 4 * 48);
#pragma unroll
      for (int fb = 0; fb < 3; ++fb)
#pragma unroll
        for (int vb = 0; vb < 2; ++vb) kv[fb][vb] = mfma(af[fb], bf[vb], kv[fb][vb]);
    }
    const int head = 8 * (cs - 11) + w;
    const unsigned rec = KVP_OFF + (head * 16 + rt) * KVP_REC;
    const unsigned o4 = rec + tok * 64 + g * 16, o2 = g < 2 ? rec + tok * 32 + g * 16 : 0x80000000u;
    const unsigned t5 = tok < 5 ? 0u : 0x80000000u;
    st16(rws, o4, __builtin_bit_cast(v4u, kv[0][0]));
    st16(rws, o4 + 1024, __builtin_bit_cast(v4u, kv[1][0]));
    st16(rws, o2 + 2048, __builtin_bit_cast(v4u, kv[2][0]));
    st16(rws, (o4 | t5) + 2560, __builtin_bit_cast(v4u, kv[0][1]));
    st16(rws, (o4 | t5) + 2880, __builtin_bit_cast(v4u, kv[1][1]));
    st16(rws, (o2 | t5) + 3200, __builtin_bit_cast(v4u, kv[2][1]));
  }
  }
  STAMP_AT(0, 3);
  STAMP_FLUSH(0);
}

constexpr unsigned L2_RED = 0, L2_REDO = 61440, L2_KVS = L2_REDO + 81920, L2_FLAG = L2_KVS + 21 * 80 + 16, K2_LDS = L2_FLAG + 64;
#define OLD_AUX 16
#define OUT_AUX 16
#define K2_P 2

struct Stage { v4u a[5], b[4]; };
DI void k2_load(Stage& st, __amdgpu_buffer_rsrc_t ra, __amdgpu_buffer_rsrc_t rw, const unsigned (&vb)[4], unsigned vo, int ks) {
#pragma unroll
  for (int i = 0; i < 4; ++i) st.b[i] = __builtin_amdgcn_raw_buffer_load_b128(ra, vb[i], ks * 4096, 0);
#pragma unroll
  for (int nb = 0; nb < 5; ++nb) st.a[nb] = __builtin_amdgcn_raw_buffer_load_b128(rw, vo + nb * 1024, ks * 5120, 0);
}

template <int NH>
DI void k2_gemm(f32x4 (&acc)[5][4], __amdgpu_buffer_rsrc_t ra, __amdgpu_buffer_rsrc_t rw, const unsigned (&vb)[4], unsigned vo, int h0) {
  Stage st[K2_P];
#pragma unroll
  for (int i = 0; i < K2_P; ++i) k2_load(st[i], ra, rw, vb, vo, h0 + i);
  SB0();
#pragma unroll
  for (int i = 0; i < NH; ++i) {
    Stage& cur = st[i % K2_P];
#pragma unroll
    for (int nb = 0; nb < 5; ++nb)
#pragma unroll
      for (int m = 0; m < 4; ++m) acc[nb][m] = mfma(cur.a[nb], cur.b[m], acc[nb][m]);
    if (i + K2_P < NH) k2_load(cur, ra, rw, vb, vo, h0 + i + K2_P);
    SB0();
  }
}

__global__ __launch_bounds__(512, 2) void k2(K2Args p) {
  extern __shared__ __attribute__((aligned(16))) char smem[];
  LAS char* L = (LAS char*)smem;
  const int tid = threadIdx.x, lane = tid & 63, w = rfl(tid >> 6);
  const int bid = blockIdx.x, xcd = bid & 7, idx = bid >> 3;
  const int g = lane >> 4, tok = lane & 15;
  char* ws = p.ws;
  STAMP_DECL;
  STAMP_AT(1, 0);
  const int rt64 = (xcd >> 1) * 8 + (idx & 7), ct = (xcd & 1) * 4 + (idx >> 3);
  const __amdgpu_buffer_rsrc_t ra = mk_rsrc(ws + ACT_OFF + rt64 * (NKS * 4096), NKS * 4096);
  const __amdgpu_buffer_rsrc_t rw = mk_rsrc(ws + WT_OFF + ct * (NKS * 5120), NKS * 5120);
  const f32x4 z4 = {0.f, 0.f, 0.f, 0.f};
  f32x4 acc[5][4];
#pragma unroll
  for (int nb = 0; nb < 5; ++nb)
#pragma unroll
    for (int m = 0; m < 4; ++m) acc[nb][m] = z4;

  if (w >= 4) {
#define EARLYBAR
#define OPRIO_LO 2
#define OPRIO 2
    __builtin_amdgcn_s_setprio(OPRIO);
    if (tid == 256) *(volatile LAS unsigned*)(L + L2_FLAG) = 0u;
    asm volatile("s_waitcnt lgkmcnt(0)\n\ts_barrier" ::: "memory");
    const __amdgpu_buffer_rsrc_t rws = mk_rsrc(ws, WS_BYTES);
    volatile LAS unsigned* flag = (volatile LAS unsigned*)(L + L2_FLAG);
    const int head = xcd * 4 + (idx & 3), rg = idx >> 2, ww = w - 4, t = tid - 256;
    v4u qf[4];
    const v4u wa0 = __builtin_amdgcn_raw_buffer_load_b128(rws, lane * 16, WPF_OFF + head * 2048, 0);
    const v4u wa1 = __builtin_amdgcn_raw_buffer_load_b128(rws, lane * 16 + 1024, WPF_OFF + head * 2048, 0);
#pragma unroll
    for (int m = 0; m < 4; ++m)
      qf[m] = __builtin_amdgcn_raw_buffer_load_b128(rws, lane * 16 + m * 1024, Q_OFF + (head * 128 + rg * 16 + ww * 4) * 1024, 0);
    f32x4 v[16];
#pragma unroll
    for (int r = 0; r < 16; ++r)
      v[r] = __builtin_bit_cast(f32x4, __builtin_amdgcn_raw_buffer_load_b128(rws, t * 16, KVP_OFF + (head * 16 + r) * KVP_REC, 0));
    SB0();
    __builtin_amdgcn_s_setprio(OPRIO_LO);
    STAMP_AT(1, 5);
    v4u fp[4], fn[4];
#pragma unroll
    for (int m = 0; m < 4; ++m) {
      const f32x4 p0 = mfma(wa0, qf[m], z4);
      f32x4 p1 = mfma(wa1, qf[m], z4);
      if (g != 0) p1 = p0;
      float mx = fmaxf(fmaxf(fmaxf(p0[0], p0[1]), fmaxf(p0[2], p0[3])), fmaxf(fmaxf(p1[0], p1[1]), fmaxf(p1[2], p1[3])));
      float mn = fminf(fminf(fminf(p0[0], p0[1]), fminf(p0[2], p0[3])), fminf(fminf(p1[0], p1[1]), fminf(p1[2], p1[3])));
      const float l2e = 1.4426950408889634f;
      const float mxs = red4_max(mx) * -l2e, mns = red4_max(-mn) * -l2e;
      const float keep = g == 0 ? 1.0f : 0.0f;
      f32x2 ep[4], en[4];
#pragma unroll
      for (int i = 0; i < 4; ++i) {
        const f32x4 s = i < 2 ? p0 : p1;
        const f32x2 x = f32x2{s[2 * (i & 1)], s[2 * (i & 1) + 1]};
        ep[i] = exp2v(x * l2e + mxs);
        en[i] = exp2v(x * -l2e + mns);
      }
      const f32x2 sp = (ep[0] + ep[1]) + (ep[2] + ep[3]) * keep, sn = (en[0] + en[1]) + (en[2] + en[3]) * keep;
      const float rp = __builtin_amdgcn_rcpf(red4_sum(sp[0] + sp[1])), rn = __builtin_amdgcn_rcpf(red4_sum(sn[0] + sn[1]));
      const float rpk = rp * keep, rnk = rn * keep;
      fp[m] = v4u{cvtpk(ep[0] * rp), cvtpk(ep[1] * rp), cvtpk(ep[2] * rpk), cvtpk(ep[3] * rpk)};
      fn[m] = v4u{cvtpk(en[0] * rn), cvtpk(en[1] * rn), cvtpk(en[2] * rnk), cvtpk(en[3] * rnk)};
    }
    SB0();
    STAMP_AT(1, 6);
    {
      f32x4 s = v[0];
#pragma unroll
      for (int r = 1; r < 16; ++r) s += v[r];
      int d, f0;
      if (t < 128) { d = (t & 63) >> 2; f0 = 16 * (t >> 6) + 4 * (t & 3); }
      else if (t < 160) { d = (t - 128) >> 1; f0 = 32 + 4 * (t & 1); }
      else if (t < 200) { const int u = t < 180 ? t - 160 : t - 180; d = 16 + (u >> 2); f0 = (t < 180 ? 0 : 16) + 4 * (u & 3); }
      else { d = 16 + ((t - 200) >> 1); f0 = 32 + 4 * (t & 1); }
      if (t < 210) *(LAS v2u*)(L + L2_KVS + d * 80 + f0 * 2) = cvt4(s);
    }
    asm volatile("s_waitcnt lgkmcnt(0)" ::: "memory");
    if (lane == 0) __hip_atomic_fetch_add((LAS unsigned*)(L + L2_FLAG), 1u, __ATOMIC_RELAXED, __HIP_MEMORY_SCOPE_WORKGROUP);
    while (flag[0] < 4u) __builtin_amdgcn_s_sleep(1);
    asm volatile("" ::: "memory");
    __builtin_amdgcn_s_setprio(OPRIO);
    STAMP_AT(1, 1);
    {
      v4u ka[2][2];
#pragma unroll
      for (int b = 0; b < 2; ++b)
#pragma unroll
        for (int st = 0; st < 2; ++st) {
          const int d = b == 0 ? tok : (tok < 4 ? 16 + tok : 20);
          LAS char* src = L + L2_KVS + d * 80 + st * 40;
          const v2u lo = *(LAS v2u*)(src + g * 8), hi = *(LAS v2u*)(src + 32);
          ka[b][st] = v4u{lo.x, lo.y, hi.x, hi.y};
        }
      const int rto = rg * 4 + ww;
      const int Q0 = 5 * head + g, Q1 = 5 * head + 4;
      const unsigned o0 = (unsigned)(ACT_OFF + (rto * NKS + NKH + (Q0 >> 3)) * 4096 + (Q0 & 7) * 256 + tok * 16);
      const unsigned o1 = g == 0 ? (unsigned)(ACT_OFF + (rto * NKS + NKH + (Q1 >> 3)) * 4096 + (Q1 & 7) * 256 + tok * 16) : 0x80000000u;
      v2u a[4], b[4];
#pragma unroll
      for (int m = 0; m < 4; ++m) {
        f32x4 n0 = mfma(ka[0][0], fp[m], z4), n1 = mfma(ka[1][0], fp[m], z4);
        n0 = mfma(ka[0][1], fn[m], n0);
        n1 = mfma(ka[1][1], fn[m], n1);
        auto r = __builtin_amdgcn_permlane16_swap(f2u(n1[0]), f2u(n1[0]), false, false);
        const unsigned r1 = r[1];
        const float sc = __builtin_amdgcn_rcpf(u2f(r1) + 1e-6f);
        a[m] = cvt4(n0 * sc); b[m] = cvt4(n1 * sc);
        a[m].x = a[m].x == SENT ? 0x7fc07fc0u : a[m].x; a[m].y = a[m].y == SENT ? 0x7fc07fc0u : a[m].y;
        b[m].x = b[m].x == SENT ? 0x7fc07fc0u : b[m].x; b[m].y = b[m].y == SENT ? 0x7fc07fc0u : b[m].y;
        if (m & 1) {
          st16(rws, o0 + (m >> 1) * 2048, v4u{a[m - 1].x, a[m - 1].y, a[m].x, a[m].y});
          st16(rws, o1 + (m >> 1) * 2048, v4u{b[m - 1].x, b[m - 1].y, b[m].x, b[m].y});
        }
      }
    }
    STAMP_AT(1, 2);
    v4u wa[5][5];
#pragma unroll
    for (int i = 0; i < 5; ++i)
#pragma unroll
      for (int nb = 0; nb < 5; ++nb) wa[i][nb] = __builtin_amdgcn_raw_buffer_load_b128(rw, lane * 16 + nb * 1024, (NKH + 5 * ww + i) * 5120, 0);
    SB0();
    {
      const int qd = lane < 40 ? lane : 39;
      const unsigned co = (unsigned)((qd >> 3) * 4096 + 2048 + (qd & 7) * 256 + 252);
      for (unsigned spins = 0; spins < (1u << 16); ++spins) {
        const unsigned cv = __builtin_amdgcn_raw_buffer_load_b32(ra, co, (NKH + 5 * ww) * 4096, 16);
        if (!__any(cv == SENT)) break;
        __builtin_amdgcn_s_sleep(4);
      }
    }
    STAMP_AT(1, 7);
    v4u ob[5][4];
    for (unsigned spins = 0;; ++spins) {
#pragma unroll
      for (int i = 0; i < 5; ++i)
#pragma unroll
        for (int m = 0; m < 4; ++m) ob[i][m] = __builtin_amdgcn_raw_buffer_load_b128(ra, lane * 16 + m * 1024, (NKH + 5 * ww + i) * 4096, OLD_AUX);
      __builtin_amdgcn_s_setprio(OPRIO_LO);
      bool bad = false;
#pragma unroll
      for (int i = 0; i < 5; ++i)
#pragma unroll
        for (int m = 0; m < 4; ++m)
          bad |= (ob[i][m].x == SENT) | (ob[i][m].y == SENT) | (ob[i][m].z == SENT) | (ob[i][m].w == SENT);
      if (!__any(bad) || spins > (1u << 12)) break;
      __builtin_amdgcn_s_sleep(8);
    }
    STAMP_AT(1, 3);
#pragma unroll
    for (int i = 0; i < 5; ++i) {
      v4u fr[4];
#pragma unroll
      for (int pr = 0; pr < 2; ++pr) {
        fr[2 * pr] = v4u{ob[i][2 * pr].x, ob[i][2 * pr].y, ob[i][2 * pr + 1].x, ob[i][2 * pr + 1].y};
        fr[2 * pr + 1] = v4u{ob[i][2 * pr].z, ob[i][2 * pr].w, ob[i][2 * pr + 1].z, ob[i][2 * pr + 1].w};
      }
#pragma unroll
      for (int nb = 0; nb < 5; ++nb)
#pragma unroll
        for (int m = 0; m < 4; ++m) acc[nb][m] = mfma(wa[i][nb], fr[m], acc[nb][m]);
    }
#pragma unroll
    for (int m = 0; m < 4; ++m)
#pragma unroll
      for (int nb = 0; nb < 5; ++nb) *(LAS f32x4*)(L + L2_REDO + ((m * 4 + ww) * 5 + nb) * 1024 + lane * 16) = acc[nb][m];
    STAMP_AT(1, 4);
    __syncthreads();
    STAMP_FLUSH(1);
    return;
  }

  unsigned vb[4];
#pragma unroll
  for (int i = 0; i < 4; ++i) vb[i] = lane * 16 + ((w + i) & 3) * 1024;
  asm volatile("s_barrier" ::: "memory");
  STAMP_AT(1, 5);
  const float gsc = *((const float*)(ws + G_OFF) + rt64 * 64 + w * 16 + tok);
  if (w < 2) k2_gemm<14>(acc, ra, rw, vb, lane * 16, 14 * w);
  else k2_gemm<13>(acc, ra, rw, vb, lane * 16, 28 + 13 * (w - 2));
  STAMP_AT(1, 1);
#pragma unroll
  for (int i = 1; i < 4; ++i) {
    LAS char* dst = L + L2_RED + (((w + i) & 3) * 3 + (i - 1)) * 5120 + lane * 16;
#pragma unroll
    for (int nb = 0; nb < 5; ++nb) *(LAS f32x4*)(dst + nb * 1024) = acc[nb][i];
  }
  __syncthreads();
  STAMP_AT(1, 2);
  f32x4 r[5];
#pragma unroll
  for (int nb = 0; nb < 5; ++nb) r[nb] = acc[nb][0];
#pragma unroll
  for (int sl = 0; sl < 3; ++sl)
#pragma unroll
    for (int nb = 0; nb < 5; ++nb) r[nb] += *(LAS f32x4*)(L + L2_RED + ((w * 3 + sl) * 5 + nb) * 1024 + lane * 16);
  f32x4 ro[5];
#pragma unroll
  for (int nb = 0; nb < 5; ++nb) ro[nb] = *(LAS f32x4*)(L + L2_REDO + ((w * 4) * 5 + nb) * 1024 + lane * 16);
#pragma unroll
  for (int sl = 1; sl < 4; ++sl)
#pragma unroll
    for (int nb = 0; nb < 5; ++nb) ro[nb] += *(LAS f32x4*)(L + L2_REDO + ((w * 4 + sl) * 5 + nb) * 1024 + lane * 16);
#pragma unroll
  for (int nb = 0; nb < 5; ++nb) r[nb] = r[nb] * (1.0f - gsc) + ro[nb] * gsc;
  const __amdgpu_buffer_rsrc_t ro_ = mk_rsrc(p.out, S_ * D_ * 2);
  const unsigned op = (unsigned)(((rt64 * 64 + w * 16 + tok) * D_ + ct * 80) * 2);
  __builtin_amdgcn_raw_buffer_store_b128(cvt8(r[0], r[1]), ro_, op + 16 * g, 0, OUT_AUX);
  __builtin_amdgcn_raw_buffer_store_b128(cvt8(r[2], r[3]), ro_, op + 64 + 16 * g, 0, OUT_AUX);
  __builtin_amdgcn_raw_buffer_store_b64(cvt4(r[4]), ro_, op + 128 + 8 * g, 0, OUT_AUX);
  STAMP_AT(1, 3);
  STAMP_FLUSH(1);
}

extern "C" void kernel_launch(void* const* d_in, const int* in_sizes, int n_in, void* d_out, int out_size, void* d_ws,
                              size_t ws_size, hipStream_t stream) {
  K1Args a;
  a.x = (const u16*)d_in[0]; a.wqkv = (const u16*)d_in[1]; a.wphi = (const u16*)d_in[2]; a.wo = (const u16*)d_in[3];
  a.wg = (const u16*)d_in[5]; a.wu = (const u16*)d_in[6]; a.wd = (const u16*)d_in[7]; a.gw = (const u16*)d_in[8];
  a.ws = (char*)d_ws;
  K2Args b;
  b.ws = a.ws; b.out = (u16*)d_out;
  (void)hipFuncSetAttribute((const void*)k1, hipFuncAttributeMaxDynamicSharedMemorySize, K1_LDS);
  (void)hipFuncSetAttribute((const void*)k2, hipFuncAttributeMaxDynamicSharedMemorySize, K2_LDS);
  k1<<<256, 768, K1_LDS, stream>>>(a);
  k2<<<256, 512, K2_LDS, stream>>>(b);
}
```

```cpp
#include <hip/hip_runtime.h>

typedef unsigned short u16;
typedef unsigned v4u __attribute__((ext_vector_type(4)));
typedef unsigned v2u __attribute__((ext_vector_type(2)));
typedef float f32x4 __attribute__((ext_vector_type(4)));
typedef float f32x2 __attribute__((ext_vector_type(2)));
typedef short s16x4 __attribute__((ext_vector_type(4)));
typedef __bf16 bf16x8 __attribute__((ext_vector_type(8)));
typedef __bf16 bf16x2 __attribute__((ext_vector_type(2)));
#define LAS __attribute__((address_space(3)))
#define GAS __attribute__((address_space(1)))
#define DI __device__ __forceinline__

constexpr int S_ = 2048, D_ = 640, F_ = 1728;
constexpr int NKS = 74, NKH = 54;
constexpr unsigned ACT_OFF = 0, ACT_BYTES = 32u * NKS * 4 * 1024;
constexpr unsigned WT_OFF = ACT_OFF + ACT_BYTES, WT_BYTES = 8u * NKS * 5 * 1024;
constexpr unsigned Q_OFF = WT_OFF + WT_BYTES, Q_BYTES = 32u * 128 * 1024;
constexpr unsigned KVP_REC = 3360;
constexpr unsigned KVP_OFF = Q_OFF + Q_BYTES, KVP_BYTES = 32u * 16 * KVP_REC;
constexpr unsigned G_OFF = KVP_OFF + KVP_BYTES, G_BYTES = S_ * 4;
constexpr unsigned WPF_OFF = G_OFF + G_BYTES, WPF_BYTES = 32u * 2 * 1024;
constexpr unsigned FL_EPOCH = WPF_OFF + WPF_BYTES, FL_RT = FL_EPOCH + 256, FL_AUX = FL_RT + 16 * 128, FL_KV = FL_AUX + 8 * 128;
constexpr unsigned FL_O = FL_KV + 8 * 128;
constexpr unsigned WS_BYTES = FL_O + 32 * 256;
constexpr unsigned SENT = 0xFFFFFFFFu;

constexpr unsigned L_W = 0, WSLOT = 24576, L_X = 4 * WSLOT, XSLOT = 16384, L_WPHI = L_X + 2 * XSLOT, L_ZERO = L_WPHI + 6400,
                   L_DUMMY = L_WPHI + 8192, K1_LDS = 163840;
constexpr unsigned L_KVS = 161792, L_CTL = 163584;
enum { C_TAG = 0, C_LOOP = 8, C_DRAIN = 12, C_X = 16, C_KV1 = 32, C_KV2 = 36, C_AUXOK = 40, C_PRE = 44, C_CONS = 48, C_OSYNC = 52, C_KVOK = 56, C_T1 = 60, C_HOK = 64, C_T2 = 68, C_OOK = 72 };
constexpr unsigned L_PK = 0, L_V = 8 * 128 * 80;

DI int fresh_lane() {
  int l;
  asm volatile("v_mbcnt_lo_u32_b32 %0, -1, 0\n\tv_mbcnt_hi_u32_b32 %0, -1, %0" : "=v"(l));
  return l;
}
#define LANE_VARS(l) const int lane = (l), g = lane >> 4, q = (lane >> 2) & 3, pp = lane & 3, tok = lane & 15; (void)q; (void)pp; (void)tok; (void)g
DI unsigned rfl(unsigned x) { return __builtin_amdgcn_readfirstlane(x); }
DI unsigned cvtpk(f32x2 v) {
  bf16x2 b = __builtin_convertvector(v, bf16x2);
  return __builtin_bit_cast(unsigned, b);
}
DI unsigned cvtpk(float lo, float hi) { return cvtpk(f32x2{lo, hi}); }
DI v2u cvt4(f32x4 v) { return v2u{cvtpk(v[0], v[1]), cvtpk(v[2], v[3])}; }
DI v4u cvt8(f32x4 a, f32x4 b) { return v4u{cvtpk(a[0], a[1]), cvtpk(a[2], a[3]), cvtpk(b[0], b[1]), cvtpk(b[2], b[3])}; }
DI float bf2f(unsigned b) { return __builtin_bit_cast(float, b << 16); }
DI f32x4 mfma(v4u a, v4u b, f32x4 c) {
  return __builtin_amdgcn_mfma_f32_16x16x32_bf16(__builtin_bit_cast(bf16x8, a), __builtin_bit_cast(bf16x8, b), c, 0, 0, 0);
}
DI v2u tr_read(LAS char* p) {
  s16x4 r = __builtin_amdgcn_ds_read_tr16_b64_v4i16((LAS s16x4*)p);
  return __builtin_bit_cast(v2u, r);
}
DI v4u tr_read2(LAS char* p, int d) {
  v2u a = tr_read(p), b = tr_read(p + d);
  return v4u{a.x, a.y, b.x, b.y};
}
DI __amdgpu_buffer_rsrc_t mk_rsrc(const void* p, unsigned bytes) {
  return __builtin_amdgcn_make_buffer_rsrc((void*)p, (short)0, (int)bytes, 0x00020000);
}
DI float u2f(unsigned u) { return __builtin_bit_cast(float, u); }
DI unsigned f2u(float f) { return __builtin_bit_cast(unsigned, f); }
DI float red4_max(float x) {
  auto r = __builtin_amdgcn_permlane16_swap(f2u(x), f2u(x), false, false);
  unsigned r0 = r[0], r1 = r[1];
  x = fmaxf(u2f(r0), u2f(r1));
  auto t = __builtin_amdgcn_permlane32_swap(f2u(x), f2u(x), false, false);
  r0 = t[0]; r1 = t[1];
  return fmaxf(u2f(r0), u2f(r1));
}
DI float red4_sum(float x) {
  auto r = __builtin_amdgcn_permlane16_swap(f2u(x), f2u(x), false, false);
  unsigned r0 = r[0], r1 = r[1];
  x = u2f(r0) + u2f(r1);
  auto t = __builtin_amdgcn_permlane32_swap(f2u(x), f2u(x), false, false);
  r0 = t[0]; r1 = t[1];
  return u2f(r0) + u2f(r1);
}
DI f32x2 exp2v(f32x2 a) { return f32x2{__builtin_amdgcn_exp2f(a[0]), __builtin_amdgcn_exp2f(a[1])}; }
DI void st16(__amdgpu_buffer_rsrc_t r, unsigned off, v4u v) { __builtin_amdgcn_raw_buffer_store_b128(v, r, off, 0, 16); }
DI void st8(__amdgpu_buffer_rsrc_t r, unsigned off, v2u v) { __builtin_amdgcn_raw_buffer_store_b64(v, r, off, 0, 16); }
#define AUX_DELAY 6
#define PF_MODE 1
#define PF_D0 8
#define PF_D1 1
#define SB0() __builtin_amdgcn_sched_barrier(0)
#define SYNC(n) do { SB0(); asm volatile("s_waitcnt vmcnt(" #n ") lgkmcnt(0)\n\ts_barrier" ::: "memory"); SB0(); } while (0)
#define NT_A "nt"
#define LDSDMA(m0v, vo, rs, so) \
  asm volatile("s_mov_b32 m0, %0\n\ts_nop 0\n\tbuffer_load_dwordx4 %1, %2, %3 offen " NT_A " lds" :: "s"(m0v), "v"(vo), "s"(rs), "s"(so) : "memory")

#define STAMP_DECL
#define STAMP_AT(kern, i)
#define STAMP_FLUSH(kern)
#define STAMP_ARG
#define STAMP_PASS

struct Args {
  const u16 *x, *wqkv, *wphi, *wo, *wg, *wu, *wd, *gw;
  char* ws;
  u16* out;
};

#define SYNCL() do { SB0(); asm volatile("s_waitcnt lgkmcnt(0)\n\ts_barrier" ::: "memory"); SB0(); } while (0)
#define NT_D ""
#define NT_P ""
#define DMA(m0v, vo, rs, so, off) \
  asm volatile("s_mov_b32 m0, %0\n\ts_nop 0\n\tbuffer_load_dwordx4 %1, %2, %3 offen offset:%4 " NT_D " lds" :: "s"(m0v), "v"(vo), "s"(rs), "s"(so), "i"(off) : "memory")
#define DMAS(m0v, vo, rs, so, off) \
  asm volatile("s_mov_b32 m0, %0\n\ts_nop 0\n\tbuffer_load_dwordx4 %1, %2, %3 offen offset:%4 sc1 lds" :: "s"(m0v), "v"(vo), "s"(rs), "s"(so), "i"(off) : "memory")

template <int MODE> struct MI { static constexpr int NB = MODE == 1 ? 5 : 6; };
template <int MODE, int B, int R, int SLOT> DI v2u a_read(LAS char* L, unsigned (&aoff)[5], LAS char* gp) {
  if constexpr (MODE == 2 && B == 5) return *(LAS v2u*)(gp + R * 8);
  else {
    constexpr int I = MODE == 0 ? B >> 1 : B;
    const v2u t = tr_read(L + L_W + (SLOT & 1) * WSLOT + aoff[I] + ((MODE == 0 && (B & 1)) ? 12288 : 0) + R * 1536);
    if constexpr (R == 1 && (MODE != 0 || (B & 1))) {
      if constexpr (SLOT == 1) asm volatile("v_add_u32 %0, 0xc000, %0" : "+v"(aoff[I]));
      if constexpr (SLOT == 3) asm volatile("v_subrev_u32 %0, 0xc000, %0" : "+v"(aoff[I]));
    }
    return t;
  }
}
DI void mfma_ip(f32x4& c, const v4u& a, const v4u& b) {
  asm volatile("v_mfma_f32_16x16x32_bf16 %0, %1, %2, %0" : "+v"(c) : "v"(a), "v"(b));
}
template <int MODE, bool NEXT, int T, int J = 0>
DI void k1_step(f32x4 (&acc)[4][MI<MODE>::NB], v4u (&a)[4], v4u (&x)[2][4], LAS char* L, unsigned (&aoff)[5],
                const unsigned (&xoff)[2], LAS char* gp) {
  constexpr int NB = MI<MODE>::NB, R0 = T * NB;
  if constexpr (J < 4 * NB) {
    constexpr int b = J >> 2, r = J & 3, nb = b + 2;
    if constexpr (r < 2) {
      if constexpr (nb < NB || NEXT) {
        v2u t = a_read<MODE, nb % NB, r, (nb < NB ? T : T + 1) & 3>(L, aoff, gp + T * 64);
        if constexpr (r == 0) { a[(R0 + nb) & 3].x = t.x; a[(R0 + nb) & 3].y = t.y; }
        else { a[(R0 + nb) & 3].z = t.x; a[(R0 + nb) & 3].w = t.y; }
      }
    } else if constexpr (r == 2 && b < 4 && NEXT)
      x[(T + 1) & 1][b] = *(LAS v4u*)(L + xoff[(T + 1) & 1] + ((((T + 1) >> 1) & 1) * XSLOT + b * 2048));
    mfma_ip(acc[r][b], a[(R0 + b) & 3], x[T & 1][r]);
    SB0();
    k1_step<MODE, NEXT, T, J + 1>(acc, a, x, L, aoff, xoff, gp);
  }
}
template <int MODE>
DI void k1_mainloop(f32x4 (&acc)[4][MI<MODE>::NB], LAS char* L, unsigned (&aoff)[5], unsigned xoff0, int g STAMP_ARG) {
  unsigned xoff[2] = {xoff0 + L_X, (xoff0 ^ 64) + L_X};
  asm volatile("" : "+v"(xoff[0]), "+v"(xoff[1]));
  v4u a[4], x[2][4];
  unsigned gpo = L_WPHI + g * 16;
  asm volatile("" : "+v"(gpo));
  LAS char* gp = L + gpo;
#pragma unroll
  for (int m = 0; m < 4; ++m)
#pragma unroll
    for (int b = 0; b < MI<MODE>::NB; ++b) acc[m][b] = f32x4{0.f, 0.f, 0.f, 0.f};
  SYNCL();
  STAMP_AT(0, 4);
#pragma unroll
  for (int m = 0; m < 4; ++m) x[0][m] = *(LAS v4u*)(L + xoff[0] + m * 2048);
  {
    const v2u t0 = a_read<MODE, 0, 0, 0>(L, aoff, gp), t1 = a_read<MODE, 0, 1, 0>(L, aoff, gp);
    const v2u t2 = a_read<MODE, 1, 0, 0>(L, aoff, gp), t3 = a_read<MODE, 1, 1, 0>(L, aoff, gp);
    a[0] = v4u{t0.x, t0.y, t1.x, t1.y}; a[1] = v4u{t2.x, t2.y, t3.x, t3.y};
  }
#pragma unroll 1
  for (int i = 0; i < 16; i += 4) {
    SYNCL(); k1_step<MODE, true, 0>(acc, a, x, L, aoff, xoff, gp);
    SYNCL(); k1_step<MODE, true, 1>(acc, a, x, L, aoff, xoff, gp);
    SYNCL(); k1_step<MODE, true, 2>(acc, a, x, L, aoff, xoff, gp);
    SYNCL(); k1_step<MODE, true, 3>(acc, a, x, L, aoff, xoff, gp);
    gp += 256;
  }
  SYNCL(); k1_step<MODE, true, 0>(acc, a, x, L, aoff, xoff, gp);
  SYNCL(); k1_step<MODE, true, 1>(acc, a, x, L, aoff, xoff, gp);
  SYNCL(); k1_step<MODE, true, 2>(acc, a, x, L, aoff, xoff, gp);
  k1_step<MODE, false, 3>(acc, a, x, L, aoff, xoff, gp);
  asm volatile("s_nop 15" ::: "memory");
  __builtin_amdgcn_s_setprio(2);
  SB0();
}

DI unsigned gelu_mul2(f32x2 x, f32x2 u) {
  const float k0 = -2.0f * 0.7978845608028654f * 1.4426950408889634f, k1 = k0 * 0.044715f;
  f32x2 t = x * x;
  f32x2 a = x * (t * k1 + k0);
  f32x2 d = exp2v(a) + 1.0f;
  f32x2 r = f32x2{__builtin_amdgcn_rcpf(d[0]), __builtin_amdgcn_rcpf(d[1])};
  return cvtpk((x * u) * r);
}
DI v2u gelu_mul4(f32x4 x, f32x4 u) {
  return v2u{gelu_mul2(f32x2{x[0], x[1]}, f32x2{u[0], u[1]}), gelu_mul2(f32x2{x[2], x[3]}, f32x2{u[2], u[3]})};
}

#define OPRIO 3
#define POLL_SLEEP 12
DI void lds_arrive(LAS char* L, unsigned word) {
  asm volatile("s_waitcnt lgkmcnt(0)" ::: "memory");
  if (fresh_lane() == 0) __hip_atomic_fetch_add((LAS unsigned*)(L + L_CTL + word), 1u, __ATOMIC_RELAXED, __HIP_MEMORY_SCOPE_WORKGROUP);
}
DI void lds_wait(LAS char* L, unsigned word, unsigned target) {
  volatile LAS unsigned* f = (volatile LAS unsigned*)(L + L_CTL + word);
  for (unsigned spins = 0; rfl(*f) < target && spins < (1u << 22); ++spins) __builtin_amdgcn_s_sleep(1);
  asm volatile("" ::: "memory");
}
DI void poll_line(__amdgpu_buffer_rsrc_t rws, unsigned line, unsigned mask, v2u tag) {
  const int l = fresh_lane() & 31;
  const bool need = (mask >> l) & 1u;
  for (unsigned spins = 0; spins < (1u << 18); ++spins) {
    const v2u v = __builtin_amdgcn_raw_buffer_load_b64(rws, l * 8, line, 16);
    if (!__any(need && (v.x != tag.x || v.y != tag.y))) break;
    __builtin_amdgcn_s_sleep(POLL_SLEEP);
  }
  __builtin_amdgcn_fence(__ATOMIC_ACQUIRE, "wavefront");
}

DI void poll_once(LAS char* L, unsigned ticket, unsigned okw, __amdgpu_buffer_rsrc_t rws, unsigned line, unsigned mask, v2u tag) {
  unsigned t = 0;
  if (fresh_lane() == 0) t = __hip_atomic_fetch_add((LAS unsigned*)(L + L_CTL + ticket), 1u, __ATOMIC_RELAXED, __HIP_MEMORY_SCOPE_WORKGROUP);
  if (rfl(t) == 0) { poll_line(rws, line, mask, tag); lds_arrive(L, okw); }
  else lds_wait(L, okw, 1);
}
DI void mfma_ip2(f32x4& c, const v4u& a, const v4u& b) {
  asm volatile("s_nop 1\n\tv_mfma_f32_16x16x32_bf16 %0, %1, %2, %0" : "+v"(c) : "v"(a), "v"(b));
}
DI void mfma_init(f32x4& c, const v4u& a, const v4u& b) {
  asm volatile("s_nop 1\n\tv_mfma_f32_16x16x32_bf16 %0, %1, %2, 0" : "=&v"(c) : "v"(a), "v"(b));
}
struct Stage { v4u a[5], b[4]; };
DI void p2_load_b(Stage& st, __amdgpu_buffer_rsrc_t ra, unsigned vo, int ks) {
#pragma unroll
  for (int i = 0; i < 4; ++i) st.b[i] = __builtin_amdgcn_raw_buffer_load_b128(ra, vo + i * 1024, ks * 4096, 16);
}
DI void p2_load_a(Stage& st, __amdgpu_buffer_rsrc_t rw, unsigned vo, int ks) {
#pragma unroll
  for (int nb = 0; nb < 5; ++nb) st.a[nb] = __builtin_amdgcn_raw_buffer_load_b128(rw, vo + nb * 1024, ks * 5120, 16);
}

__global__ __launch_bounds__(768, 3) void fused(Args p) {
  extern __shared__ __attribute__((aligned(16))) char smem[];
  LAS char* L = (LAS char*)smem;
  const int w = rfl(threadIdx.x >> 6);
  const int bid = blockIdx.x, xcd = bid & 7, idx = bid >> 3;
  const unsigned long long CSMAP = 0xDC87BA65E943F210ull;
  const int rt = (xcd & 1) * 8 + (idx & 7), cs = (int)(CSMAP >> (4 * ((xcd >> 1) * 4 + (idx >> 3)))) & 15;
  char* ws = p.ws;
  const __amdgpu_buffer_rsrc_t rws = mk_rsrc(ws, WS_BYTES);
  STAMP_DECL;
  STAMP_AT(0, 0);
  const bool isAUX = cs == 15, isGU = cs < 9, isKV = cs >= 11 && cs < 15;

  if (w == 0) {
    const int lane = fresh_lane();
    const v2u e = __builtin_amdgcn_raw_buffer_load_b64(rws, 0, FL_EPOCH, 16);
    unsigned long long t = (((unsigned long long)e.y << 32) | e.x) * 0x9E3779B97F4A7C15ull + 0xD1B54A32D192ED03ull;
    t ^= t >> 29;
    if (lane < 32) *(LAS unsigned*)(L + L_CTL + lane * 4) = lane == 0 ? (unsigned)t : lane == 1 ? (unsigned)(t >> 32) : 0u;
  }

  if (isAUX) {
    LANE_VARS(fresh_lane());
    if (w >= 8) {
      const int head = 2 * rt + ((w >> 1) & 1), f = 16 * (w & 1) + tok;
      const u16* wp = p.wphi + head * 400 + (f < 20 ? f : 0);
      unsigned e[5];
#pragma unroll
      for (int j = 0; j < 4; ++j) e[j] = wp[(4 * g + j) * 20];
      e[4] = wp[(16 + g) * 20];
      v4u fr = v4u{e[0] | e[1] << 16, e[2] | e[3] << 16, e[4], 0u};
      if (f >= 20) fr = v4u{0u, 0u, 0u, 0u};
      st16(rws, WPF_OFF + (head * 2 + (w & 1)) * 1024 + lane * 16, fr);
      SYNC(0); SYNC(0); SYNC(0); SYNC(0); SYNC(0);
    } else {
      const __amdgpu_buffer_rsrc_t rd = mk_rsrc(p.wd, F_ * D_ * 2), ro = mk_rsrc(p.wo, D_ * D_ * 2);
      unsigned vlo[5];
#pragma unroll
      for (int j = 0; j < 5; ++j) {
        const int Lc = ((w & 3) * 5 + j) * 64 + lane;
        vlo[j] = (unsigned)((Lc / 80) * 1280 + (Lc % 80) * 16);
      }
      auto issue = [&](int sl) {
        const int s = rt + 16 * sl;
        int b0, b1;
        if (s < NKH) {
          const int u = s / 6, e = s % 6;
          if (e < 4) { b0 = 192 * u + 48 * e; b1 = b0 + 16; } else { b0 = 192 * u + 96 * (e - 4) + 32; b1 = b0 + 48; }
        } else { b0 = 32 * (s - NKH); b1 = b0 + 16; }
        const __amdgpu_buffer_rsrc_t rs = s < NKH ? rd : ro;
        const unsigned so = (unsigned)((w < 4 ? b0 : b1) * 1280), m0 = (unsigned)((sl % 3) * 40960 + w * 5120);
#pragma unroll
        for (int j = 0; j < 5; ++j) LDSDMA(m0 + j * 1024, vlo[j], rs, so);
      };
      auto retile = [&](int sl) {
        const int s = rt + 16 * sl;
        LAS char* base = L + (sl % 3) * 40960 + (4 * g + q) * 1280 + w * 160;
        const unsigned dst = s < NKS ? WT_OFF + (w * NKS + s) * 5120 + lane * 16 : 0x80000000u;
#pragma unroll
        for (int nb = 0; nb < 5; ++nb) {
          const int col = nb < 4 ? 32 * (nb >> 1) + 8 * pp + 4 * (nb & 1) : 64 + 4 * pp;
          st16(rws, dst + nb * 1024, tr_read2(base + col * 2, 16 * 1280));
        }
      };
      for (int i = 0; i < AUX_DELAY; ++i) __builtin_amdgcn_s_sleep(16);
      issue(0); issue(1); issue(2);
      SYNC(10); retile(0);
      SYNC(10); issue(3); retile(1);
      SYNC(15); issue(4); retile(2);
      SYNC(15); retile(3);
      SYNC(10); retile(4);
    }
  } else {
    const __amdgpu_buffer_rsrc_t rx = mk_rsrc(p.x + rt * 128 * D_, (unsigned)((S_ - rt * 128) * D_ * 2));
    const unsigned pitch = isGU ? F_ * 2 : 3 * D_ * 2, total = D_ * pitch;
    unsigned boffA, boffB;
    if (isGU) boffA = boffB = 384u * cs;
    else if (!isKV) { boffA = 640u * (cs - 9); boffB = boffA + 256u; }
    else { const int j = cs - 11; boffA = 1280u + 320u * j - ((j & 1) ? 64u : 0u); boffB = boffA + 1280u; }
    const char* baseA = (const char*)(isGU ? p.wg : p.wqkv) + boffA;
    const char* baseB = (const char*)(isGU ? p.wu : p.wqkv) + boffB;
    const int mh = w & 1, n = w >> 1;
    const int rt64 = rt * 2 + mh;
    if (w >= 8) {
      __builtin_amdgcn_s_setprio(3);
      const int lane = fresh_lane();
      const int lw = w - 8, sub = lw >> 1;
      const int r0 = 32 * lw + (lane >> 3);
      const unsigned vx0 = (unsigned)(r0 * 1280 + (((lane & 7) ^ ((r0 >> 1) & 7)) * 16)), vx1 = (vx0 ^ 64u) + 8 * 1280 - 1024;
      const unsigned m0x = L_X + lw * 4096;
#define LOAD_X(stage, slot, e) do { const unsigned m0_ = m0x + (unsigned)(slot) * XSLOT + (e) * 2048, so_ = (unsigned)(stage) * 128 + (e) * 16 * 1280; \
      DMA(m0_, vx0, rx, so_, 0); DMA(m0_, vx1, rx, so_, 1024); } while (0)
#define LOAD_XX(stage) do { LOAD_X(stage, (stage) & 1, 0); LOAD_X(stage, (stage) & 1, 1); } while (0)
      if (cs == 9 && lw < 2) {
        const __amdgpu_buffer_rsrc_t rg = mk_rsrc(p.gw, D_ * 2);
        DMA(L_WPHI + lw * 1024, (unsigned)(lw * 1024 + lane * 16), rg, 0, 0);
      }
      LOAD_XX(0);
      const __amdgpu_buffer_rsrc_t rw = mk_rsrc((sub ? baseB : baseA) + (lw & 1) * 16 * pitch, total - (sub ? boffB : boffA) - (lw & 1) * 16 * pitch);
      unsigned vw[6];
#pragma unroll
      for (int j = 0; j < 3; ++j) {
        const int Lc = 64 * j + lane, kr = Lc / 24, pos = Lc % 24;
        const unsigned c0 = (unsigned)((pos >> 3) * 128 + (((pos & 7) ^ (kr & 2)) * 16));
        vw[j] = (unsigned)kr * pitch + c0 - 1024u * j;
        vw[3 + j] = (unsigned)(kr + 8) * pitch + (c0 ^ 64u) - 1024u * j;
      }
      const unsigned m0w = L_W + sub * 12288 + (lw & 1) * 6144, wstep = 32 * pitch;
#define LOAD_W(s) do { const unsigned m0_ = m0w + (unsigned)((s) & 3) * WSLOT, so_ = (unsigned)(s) * wstep; \
      DMA(m0_, vw[0], rw, so_, 0); DMA(m0_, vw[1], rw, so_, 1024); DMA(m0_, vw[2], rw, so_, 2048); \
      DMA(m0_ + 3072, vw[3], rw, so_, 0); DMA(m0_ + 3072, vw[4], rw, so_, 1024); DMA(m0_ + 3072, vw[5], rw, so_, 2048); } while (0)
      const __amdgpu_buffer_rsrc_t rpf = lw == 3 ? rx : mk_rsrc(((rt & 1) ? baseB : baseA) + 8 * (rt >> 1) * pitch, total - ((rt & 1) ? boffB : boffA) - 8 * (rt >> 1) * pitch);
      const unsigned vpf = lw == 3 ? (unsigned)((8 * cs + (lane >> 3)) * 1280 + (lane & 7) * 16) : lw == 0 ? vw[0] : lw == 1 ? vw[1] + 1024u : vw[2] + 2048u;
      const unsigned pfstep = lw == 3 ? 128u : 64u * pitch;
#define PF(stage) DMA(L_DUMMY + lw * 1024, vpf, rpf, (unsigned)(stage) * pfstep, 0)
      LOAD_W(0);
      LOAD_W(1); LOAD_XX(1);
      LOAD_W(2); PF(2); PF(3);
      SYNC(18);
      SYNC(8); LOAD_W(3); PF(4);
      SYNC(9); LOAD_XX(2); LOAD_W(4); PF(5);
      SYNC(12); LOAD_W(5); PF(6);
      SYNC(8); LOAD_XX(3); LOAD_W(6); PF(7);
      SYNC(12); LOAD_W(7); PF(8);
      SYNC(8); LOAD_XX(4); LOAD_W(8); PF(9);
      SYNC(12); LOAD_W(9);
      SYNC(7); LOAD_XX(5); LOAD_W(10);
      SYNC(10); LOAD_W(11);
#pragma unroll 1
      for (int i = 9; i < 15; i += 2) {
        SYNC(6); LOAD_X((i + 3) >> 1, ((i + 3) >> 1) & 1, 0); LOAD_X((i + 3) >> 1, ((i + 3) >> 1) & 1, 1); LOAD_W(i + 3);
        SYNC(10); LOAD_W(i + 4);
      }
      SYNC(6); LOAD_XX(9); LOAD_W(18);
      SYNC(10); LOAD_W(19);
      if (isKV) {
        const __amdgpu_buffer_rsrc_t rp = mk_rsrc(p.wphi + 8 * (cs - 11) * 400, 6400);
        DMA(L_WPHI + lw * 2048, (unsigned)(lw * 2048 + lane * 16), rp, 0, 0);
        DMA(L_WPHI + lw * 2048, (unsigned)(lw * 2048 + lane * 16), rp, 0, 1024);
      }
      SYNC(6);
      SYNC(0);
    } else if (isGU) {
#define FRAG_VARS LANE_VARS(fresh_lane()); const int sw = ((q >> 1) & 1) | ((g & 1) << 1); \
  const unsigned xoff0 = (unsigned)((64 * mh + tok) * 128 + ((g ^ (tok >> 1)) * 16))
      f32x4 acc[4][6];
      {
        FRAG_VARS;
        unsigned aoff[5];
#pragma unroll
        for (int b = 0; b < 3; ++b) {
          const int ch = 6 * n + 2 * b + (pp >> 1);
          aoff[b] = (unsigned)((8 * g + q) * 384 + (ch >> 3) * 128 + (((ch & 7) ^ (2 * sw)) * 16) + (pp & 1) * 8);
        }
        aoff[3] = aoff[4] = 0;
        k1_mainloop<0>(acc, L, aoff, xoff0, g STAMP_PASS);
      }
      STAMP_AT(0, 1);
      const int lane = fresh_lane();
      const unsigned actA = ACT_OFF + ((rt64 * NKS + 6 * cs + n) * 4) * 1024 + lane * 16;
      const unsigned actB = ACT_OFF + ((rt64 * NKS + 6 * cs + 4 + (n >> 1)) * 4 + 2 * (n & 1)) * 1024 + lane * 16;
      v2u h2[4];
#pragma unroll
      for (int m = 0; m < 4; ++m) h2[m] = gelu_mul4(acc[m][4], acc[m][5]);
      const bool odd = n & 1;
      *(LAS v4u*)(L + L_WPHI + w * 1024 + lane * 16) = odd ? v4u{h2[0].x, h2[0].y, h2[1].x, h2[1].y} : v4u{h2[2].x, h2[2].y, h2[3].x, h2[3].y};
      const unsigned xw = C_X + 4 * ((w & 1) | ((w >> 2) << 1));
      lds_arrive(L, xw);
      const v2u k0 = odd ? h2[2] : h2[0], k1 = odd ? h2[3] : h2[1];
#pragma unroll
      for (int m = 0; m < 4; ++m) {
        const v2u h0 = gelu_mul4(acc[m][0], acc[m][1]), h1 = gelu_mul4(acc[m][2], acc[m][3]);
        st16(rws, actA + m * 1024, v4u{h0.x, h0.y, h1.x, h1.y});
        if (m == 2) {
          lds_wait(L, xw, 2);
          const v4u o = *(LAS v4u*)(L + L_WPHI + (w ^ 2) * 1024 + lane * 16);
          st16(rws, actB, odd ? v4u{o.x, o.y, k0.x, k0.y} : v4u{k0.x, k0.y, o.x, o.y});
          st16(rws, actB + 1024, odd ? v4u{o.z, o.w, k1.x, k1.y} : v4u{k1.x, k1.y, o.z, o.w});
        }
      }
    } else {
#define QKV_AOFF \
    FRAG_VARS; \
    unsigned aoff[5]; \
    { \
      const int offch = isKV ? ((cs - 11) & 1) * 4 : (n >> 1) * 4; \
      _Pragma("unroll") for (int b = 0; b < 5; ++b) { \
        const int u = 2 * offch + 20 * (n & 1) + 5 * pp + b, ch = u >> 1; \
        aoff[b] = (unsigned)((n >> 1) * 12288 + (8 * g + q) * 384 + (ch >> 3) * 128 + (((ch & 7) ^ (2 * sw)) * 16) + (u & 1) * 8); \
      } \
    }
      v4u c01[4], c23[4];
      v2u c4[4];
      if (cs == 9) {
        f32x4 acc[4][6];
        {
          QKV_AOFF
          k1_mainloop<2>(acc, L, aoff, xoff0, g STAMP_PASS);
        }
#pragma unroll
        for (int m = 0; m < 4; ++m) { c01[m] = cvt8(acc[m][0], acc[m][1]); c23[m] = cvt8(acc[m][2], acc[m][3]); c4[m] = cvt4(acc[m][4]); }
        if (n == 0) {
          LANE_VARS(fresh_lane());
          const float a = g == 0 ? acc[0][5][0] : g == 1 ? acc[1][5][0] : g == 2 ? acc[2][5][0] : acc[3][5][0];
          __builtin_amdgcn_raw_buffer_store_b32(f2u(1.0f / (1.0f + __expf(-a))), rws, G_OFF + (rt64 * 64 + lane) * 4, 0, 16);
        }
      } else {
        f32x4 acc[4][5];
        {
          QKV_AOFF
          k1_mainloop<1>(acc, L, aoff, xoff0, g STAMP_PASS);
        }
#pragma unroll
        for (int m = 0; m < 4; ++m) { c01[m] = cvt8(acc[m][0], acc[m][1]); c23[m] = cvt8(acc[m][2], acc[m][3]); c4[m] = cvt4(acc[m][4]); }
      }
      STAMP_AT(0, 1);
      LANE_VARS(fresh_lane());
      if (!isKV) {
          const int head = 16 * (cs - 9) + 4 * n + g;
        const unsigned qp = Q_OFF + (head * 128 + rt64 * 4) * 1024 + tok * 16;
#pragma unroll
        for (int m = 0; m < 4; ++m) {
          st16(rws, qp + m * 1024, v4u{c01[m].x, c01[m].y, c4[m].x & 0xffffu, 0u});
          st16(rws, qp + m * 1024 + 256, v4u{c01[m].z, c01[m].w, c4[m].x >> 16, 0u});
          st16(rws, qp + m * 1024 + 512, v4u{c23[m].x, c23[m].y, c4[m].y & 0xffffu, 0u});
          st16(rws, qp + m * 1024 + 768, v4u{c23[m].z, c23[m].w, c4[m].y >> 16, 0u});
        }
      } else {
        const int hl = 4 * (n & 1) + g;
        LAS char* xch = L + L_DUMMY + (w & 3) * 5120;
        if (n < 2) {
#pragma unroll
          for (int mm = 0; mm < 2; ++mm) {
            *(LAS v4u*)(xch + mm * 2560 + lane * 16) = c01[2 + mm];
            *(LAS v4u*)(xch + mm * 2560 + 1024 + lane * 16) = c23[2 + mm];
            *(LAS v2u*)(xch + mm * 2560 + 2048 + lane * 8) = c4[2 + mm];
          }
        }
        lds_arrive(L, C_KV1);
        v4u wf[5][3];
        {
          LAS char* wb = L + (pp == g ? L_WPHI + (unsigned)(hl * 800 + q * 40) : L_ZERO);
#pragma unroll
          for (int bo = 0; bo < 5; ++bo)
#pragma unroll
            for (int s3 = 0; s3 < 3; ++s3) {
              v2u lo = tr_read(wb + s3 * 320 + bo * 8), hi = v2u{0u, 0u};
              if (s3 < 2) hi = tr_read(wb + s3 * 320 + 160 + bo * 8);
              wf[bo][s3] = v4u{lo.x, lo.y, hi.x, hi.y};
            }
        }
        lds_wait(L, C_KV1, 8);
        if (n >= 2) {
#pragma unroll
          for (int m = 0; m < 4; ++m) {
            LAS char* dst = L + L_V + (hl * 128 + 64 * mh + 16 * m + tok) * 48;
            *(LAS v4u*)(dst) = c01[m];
            *(LAS v4u*)(dst + 16) = c23[m];
            *(LAS v4u*)(dst + 32) = v4u{c4[m].x, c4[m].y, 0x00003f80u, 0u};
          }
#pragma unroll
          for (int mm = 0; mm < 2; ++mm) {
            c01[mm] = *(LAS v4u*)(xch + mm * 2560 + lane * 16);
            c23[mm] = *(LAS v4u*)(xch + mm * 2560 + 1024 + lane * 16);
            c4[mm] = *(LAS v2u*)(xch + mm * 2560 + 2048 + lane * 8);
          }
        }
        {
          LAS char* dst0 = L + L_PK + (hl * 128 + 64 * mh + (n >= 2 ? 32 : 0) + tok) * 80;
          f32x4 pk[2][5];
#pragma unroll
          for (int m = 0; m < 2; ++m) {
            const v4u kf[3] = {c01[m], c23[m], v4u{c4[m].x, c4[m].y, 0u, 0u}};
#pragma unroll
            for (int bo = 0; bo < 5; ++bo) {
              pk[m][bo] = f32x4{0.f, 0.f, 0.f, 0.f};
#pragma unroll
              for (int s3 = 0; s3 < 3; ++s3) pk[m][bo] = mfma(wf[bo][s3], kf[s3], pk[m][bo]);
            }
          }
#pragma unroll
          for (int m = 0; m < 2; ++m) {
            float mx = pk[m][0][0], mn = pk[m][0][0];
#pragma unroll
            for (int bo = 0; bo < 5; ++bo)
#pragma unroll
              for (int r = 0; r < 4; ++r) { mx = fmaxf(mx, pk[m][bo][r]); mn = fminf(mn, pk[m][bo][r]); }
            const float l2e = 1.4426950408889634f;
            const float mxs = -mx * l2e, mns = mn * l2e;
            f32x2 ep[10], en[10];
#pragma unroll
            for (int i = 0; i < 10; ++i) {
              const f32x2 v = f32x2{pk[m][i >> 1][2 * (i & 1)], pk[m][i >> 1][2 * (i & 1) + 1]};
              ep[i] = v * l2e + mxs;
              en[i] = v * -l2e + mns;
            }
#pragma unroll
            for (int i = 0; i < 10; ++i) { ep[i] = exp2v(ep[i]); en[i] = exp2v(en[i]); }
            const f32x2 sp = (((ep[0] + ep[1]) + (ep[2] + ep[3])) + ((ep[4] + ep[5]) + (ep[6] + ep[7]))) + (ep[8] + ep[9]);
            const f32x2 sn = (((en[0] + en[1]) + (en[2] + en[3])) + ((en[4] + en[5]) + (en[6] + en[7]))) + (en[8] + en[9]);
            const float rp = __builtin_amdgcn_rcpf(sp[0] + sp[1]), rn = __builtin_amdgcn_rcpf(sn[0] + sn[1]);
            unsigned o[20];
#pragma unroll
            for (int i = 0; i < 10; ++i) { o[i] = cvtpk(ep[i] * rp); o[10 + i] = cvtpk(en[i] * rn); }
            LAS char* dst = dst0 + m * 16 * 80;
#pragma unroll
            for (int i = 0; i < 5; ++i) *(LAS v4u*)(dst + i * 16) = v4u{o[4 * i], o[4 * i + 1], o[4 * i + 2], o[4 * i + 3]};
          }
        }
        lds_arrive(L, C_KV2);
        lds_wait(L, C_KV2, 8);
        {
          f32x4 kv[3][2];
#pragma unroll
          for (int fb = 0; fb < 3; ++fb) { kv[fb][0] = f32x4{0.f, 0.f, 0.f, 0.f}; kv[fb][1] = f32x4{0.f, 0.f, 0.f, 0.f}; }
          LAS char* pa = L + L_PK + (w * 128 + 8 * g + q) * 80 + pp * 8;
          LAS char* pb = L + L_V + (w * 128 + 8 * g + q) * 48 + pp * 8;
#pragma unroll
          for (int ks = 0; ks < 4; ++ks) {
            v4u af[3], bf[2];
#pragma unroll
            for (int fb = 0; fb < 3; ++fb) af[fb] = tr_read2(pa + ks * 32 * 80 + fb * 32, 4 * 80);
#pragma unroll
            for (int vb = 0; vb < 2; ++vb) bf[vb] = tr_read2(pb + ks * 32 * 48 + vb * 32, 4 * 48);
#pragma unroll
            for (int fb = 0; fb < 3; ++fb)
#pragma unroll
              for (int vb = 0; vb < 2; ++vb) kv[fb][vb] = mfma(af[fb], bf[vb], kv[fb][vb]);
          }
          const int head = 8 * (cs - 11) + w;
          const unsigned rec = KVP_OFF + (head * 16 + rt) * KVP_REC;
          const unsigned o4 = rec + tok * 64 + g * 16, o2 = g < 2 ? rec + tok * 32 + g * 16 : 0x80000000u;
          const unsigned t5 = tok < 5 ? 0u : 0x80000000u;
          st16(rws, o4, __builtin_bit_cast(v4u, kv[0][0]));
          st16(rws, o4 + 1024, __builtin_bit_cast(v4u, kv[1][0]));
          st16(rws, o2 + 2048, __builtin_bit_cast(v4u, kv[2][0]));
          st16(rws, (o4 | t5) + 2560, __builtin_bit_cast(v4u, kv[0][1]));
          st16(rws, (o4 | t5) + 2880, __builtin_bit_cast(v4u, kv[1][1]));
          st16(rws, (o2 | t5) + 3200, __builtin_bit_cast(v4u, kv[2][1]));
            }
      }
    }
  }
  STAMP_AT(0, 2);

  const v2u tagv = *(LAS v2u*)(L + L_CTL + C_TAG);
  const v2u tag = v2u{rfl(tagv.x), rfl(tagv.y)};
  const int rt64 = (xcd >> 1) * 8 + (idx & 7), ct = (xcd & 1) * 4 + (idx >> 3);
  const __amdgpu_buffer_rsrc_t rw = mk_rsrc(ws + WT_OFF + ct * (NKS * 5120), NKH * 5120);
  const f32x4 z4 = {0.f, 0.f, 0.f, 0.f};

  if (w >= 8) {
    LANE_VARS(fresh_lane());
    const unsigned vo = lane * 16;
    const int lw = w - 8;
    __builtin_amdgcn_s_setprio(1);
    if (lw == 0) { poll_line(rws, FL_AUX + xcd * 128, 0xffffu, tag); lds_arrive(L, C_AUXOK); }
    else lds_wait(L, C_AUXOK, 1);
    STAMP_AT(1, 3);
    const int head = xcd * 4 + (idx & 3), rg = idx >> 2, t = lw * 64 + lane;
    poll_line(rws, FL_RT + (2 * rg + (lw >> 1)) * 128, 0x600u, tag);
    v4u qf[4];
    const v4u wa0 = __builtin_amdgcn_raw_buffer_load_b128(rws, vo, WPF_OFF + head * 2048, 16);
    const v4u wa1 = __builtin_amdgcn_raw_buffer_load_b128(rws, vo + 1024, WPF_OFF + head * 2048, 16);
#pragma unroll
    for (int m = 0; m < 4; ++m)
      qf[m] = __builtin_amdgcn_raw_buffer_load_b128(rws, vo + m * 1024, Q_OFF + (head * 128 + rg * 16 + lw * 4) * 1024, 16);
    const float gl = u2f(__builtin_amdgcn_raw_buffer_load_b32(rws, lane * 4, G_OFF + (rg * 256 + lw * 64) * 4, 16));
    v4u fp[4], fn[4];
#pragma unroll
    for (int m = 0; m < 4; ++m) {
      const f32x4 p0 = mfma(wa0, qf[m], z4);
      f32x4 p1 = mfma(wa1, qf[m], z4);
      if (g != 0) p1 = p0;
      float mx = fmaxf(fmaxf(fmaxf(p0[0], p0[1]), fmaxf(p0[2], p0[3])), fmaxf(fmaxf(p1[0], p1[1]), fmaxf(p1[2], p1[3])));
      float mn = fminf(fminf(fminf(p0[0], p0[1]), fminf(p0[2], p0[3])), fminf(fminf(p1[0], p1[1]), fminf(p1[2], p1[3])));
      const float l2e = 1.4426950408889634f;
      const float mxs = red4_max(mx) * -l2e, mns = red4_max(-mn) * -l2e;
      const float keep = g == 0 ? 1.0f : 0.0f;
      f32x2 ep[4], en[4];
#pragma unroll
      for (int i = 0; i < 4; ++i) {
        const f32x4 s = i < 2 ? p0 : p1;
        const f32x2 x = f32x2{s[2 * (i & 1)], s[2 * (i & 1) + 1]};
        ep[i] = exp2v(x * l2e + mxs);
        en[i] = exp2v(x * -l2e + mns);
      }
      const f32x2 sp = (ep[0] + ep[1]) + (ep[2] + ep[3]) * keep, sn = (en[0] + en[1]) + (en[2] + en[3]) * keep;
      const float rp = __builtin_amdgcn_rcpf(red4_sum(sp[0] + sp[1])), rn = __builtin_amdgcn_rcpf(red4_sum(sn[0] + sn[1]));
      const float rpk = rp * keep, rnk = rn * keep;
      fp[m] = v4u{cvtpk(ep[0] * rp), cvtpk(ep[1] * rp), cvtpk(ep[2] * rpk), cvtpk(ep[3] * rpk)};
      fn[m] = v4u{cvtpk(en[0] * rn), cvtpk(en[1] * rn), cvtpk(en[2] * rnk), cvtpk(en[3] * rnk)};
    }
    SB0();
    __builtin_amdgcn_s_setprio(OPRIO);
    STAMP_AT(1, 4);
    if (lw == 0) { poll_line(rws, FL_KV + xcd * 128, 0xffffu, tag); lds_arrive(L, C_KVOK); }
    else lds_wait(L, C_KVOK, 1);
    STAMP_AT(1, 5);
    {
      f32x4 v[16];
#pragma unroll
      for (int r = 0; r < 16; ++r)
        v[r] = __builtin_bit_cast(f32x4, __builtin_amdgcn_raw_buffer_load_b128(rws, t * 16, KVP_OFF + (head * 16 + r) * KVP_REC, 16));
      f32x4 s = v[0];
#pragma unroll
      for (int r = 1; r < 16; ++r) s += v[r];
      int d, f0;
      if (t < 128) { d = (t & 63) >> 2; f0 = 16 * (t >> 6) + 4 * (t & 3); }
      else if (t < 160) { d = (t - 128) >> 1; f0 = 32 + 4 * (t & 1); }
      else if (t < 200) { const int u = t < 180 ? t - 160 : t - 180; d = 16 + (u >> 2); f0 = (t < 180 ? 0 : 16) + 4 * (u & 3); }
      else { d = 16 + ((t - 200) >> 1); f0 = 32 + 4 * (t & 1); }
      if (t < 210) *(LAS v2u*)(L + L_KVS + d * 80 + f0 * 2) = cvt4(s);
    }
    lds_arrive(L, C_OSYNC);
    lds_wait(L, C_OSYNC, 4);
    STAMP_AT(1, 6);
    {
      v4u ka[2][2];
#pragma unroll
      for (int b = 0; b < 2; ++b)
#pragma unroll
        for (int st = 0; st < 2; ++st) {
          const int d = b == 0 ? tok : (tok < 4 ? 16 + tok : 20);
          LAS char* src = L + L_KVS + d * 80 + st * 40;
          const v2u lo = *(LAS v2u*)(src + g * 8), hi = *(LAS v2u*)(src + 32);
          ka[b][st] = v4u{lo.x, lo.y, hi.x, hi.y};
        }
      const int rto = rg * 4 + lw;
      const int Q0 = 5 * head + g, Q1 = 5 * head + 4;
      const unsigned o0 = (unsigned)(ACT_OFF + (rto * NKS + NKH + (Q0 >> 3)) * 4096 + (Q0 & 7) * 256 + tok * 16);
      const unsigned o1 = g == 0 ? (unsigned)(ACT_OFF + (rto * NKS + NKH + (Q1 >> 3)) * 4096 + (Q1 & 7) * 256 + tok * 16) : 0x80000000u;
      v2u a[4], b[4];
#pragma unroll
      for (int m = 0; m < 4; ++m) {
        f32x4 n0 = mfma(ka[0][0], fp[m], z4), n1 = mfma(ka[1][0], fp[m], z4);
        n0 = mfma(ka[0][1], fn[m], n0);
        n1 = mfma(ka[1][1], fn[m], n1);
        auto r = __builtin_amdgcn_permlane16_swap(f2u(n1[0]), f2u(n1[0]), false, false);
        const unsigned r1 = r[1];
        const float gm = u2f(__builtin_amdgcn_ds_bpermute((m * 16 + tok) * 4, f2u(gl)));
        const float sc = gm * __builtin_amdgcn_rcpf(u2f(r1) + 1e-6f);
        a[m] = cvt4(n0 * sc); b[m] = cvt4(n1 * sc);
        if (m & 1) {
          st16(rws, o0 + (m >> 1) * 2048, v4u{a[m - 1].x, a[m - 1].y, a[m].x, a[m].y});
          st16(rws, o1 + (m >> 1) * 2048, v4u{b[m - 1].x, b[m - 1].y, b[m].x, b[m].y});
        }
      }
    }
    asm volatile("s_waitcnt vmcnt(0)" ::: "memory");
    if (lane == 0) st8(rws, FL_O + (rg * 4 + lw) * 256 + head * 8, tag);
    STAMP_AT(1, 7);
    if (lw == 1) { poll_line(rws, FL_O + rt64 * 256, 0xffffffffu, tag); lds_arrive(L, C_OOK); }
    if (bid == 0 && lw == 0) {
      for (unsigned spins = 0; spins < (1u << 18); ++spins) {
        bool ok = true;
#pragma unroll
        for (int i = 0; i < 2; ++i) {
          const v4u v = __builtin_amdgcn_raw_buffer_load_b128(rws, vo + i * 1024, FL_RT, 16);
          ok &= v.x == tag.x && v.y == tag.y && v.z == tag.x && v.w == tag.y;
        }
        if (__all(ok)) break;
        __builtin_amdgcn_s_sleep(POLL_SLEEP);
      }
      if (lane == 0) st8(rws, FL_EPOCH, tag);
    }
    STAMP_FLUSH(1);
    return;
  }

  asm volatile("s_waitcnt vmcnt(0) lgkmcnt(0)" ::: "memory");
  {
    const int lane = fresh_lane();
    unsigned old = 0;
    if (lane == 0) old = __hip_atomic_fetch_add((LAS unsigned*)(L + L_CTL + C_DRAIN), 1u, __ATOMIC_RELAXED, __HIP_MEMORY_SCOPE_WORKGROUP);
    if (rfl(old) == 7u) {
      unsigned off = 0x80000000u;
      if (lane == 0) off = FL_RT + rt * 128 + cs * 8;
      else if (isAUX && lane <= 8) off = FL_AUX + (lane - 1) * 128 + rt * 8;
      else if (isKV && lane <= 2) off = FL_KV + ((cs - 11) * 2 + lane - 1) * 128 + rt * 8;
      st8(rws, off, tag);
    }
  }
  __builtin_amdgcn_s_setprio(0);
  STAMP_AT(0, 3);

  const __amdgpu_buffer_rsrc_t ra = mk_rsrc(ws + ACT_OFF + rt64 * (NKS * 4096), NKH * 4096);
  const __amdgpu_buffer_rsrc_t rao = mk_rsrc(ws + ACT_OFF + (rt64 * NKS + NKH) * 4096, (NKS - NKH) * 4096);
  const __amdgpu_buffer_rsrc_t rwo = mk_rsrc(ws + WT_OFF + (ct * NKS + NKH) * 5120, (NKS - NKH) * 5120);
  const int h0 = 7 * w, o0 = 3 * (7 - w);
  const unsigned vo = fresh_lane() * 16;
  Stage st[2];
  if (w == 0) {
    __builtin_amdgcn_s_setprio(3);
    poll_line(rws, FL_RT + (rt64 >> 1) * 128, 0x3ffu, tag);
    lds_arrive(L, C_HOK);
    __builtin_amdgcn_s_setprio(0);
  }
  f32x4 acc[5][4];
  lds_wait(L, C_AUXOK, 1);
  lds_wait(L, C_HOK, 1);
  STAMP_AT(0, 5);
  p2_load_b(st[0], ra, vo, h0);
  p2_load_a(st[0], rw, vo, h0);
  p2_load_b(st[1], ra, vo, h0 + 1);
  p2_load_a(st[1], rw, vo, h0 + 1);
  float gl = 0.f;
  SB0();
#pragma unroll
  for (int i = 0; i < 10; ++i) {
    Stage& cur = st[i & 1];
    if (i < 7) {
#pragma unroll
      for (int nb = 0; nb < 5; ++nb)
#pragma unroll
        for (int m = 0; m < 4; ++m) {
          if (i == 0) mfma_init(acc[nb][m], cur.a[nb], cur.b[m]);
          else mfma_ip2(acc[nb][m], cur.a[nb], cur.b[m]);
        }
      if (i == 6) {
        SB0();
        asm volatile("s_nop 15" ::: "memory");
        SB0();
        const int tk4 = (fresh_lane() & 15) * 4;
#pragma unroll
        for (int m = 0; m < 4; ++m) {
          const float gh = 1.0f - u2f(__builtin_amdgcn_ds_bpermute(m * 64 + tk4, f2u(gl)));
#pragma unroll
          for (int nb = 0; nb < 5; ++nb) acc[nb][m] *= gh;
        }
        SB0();
      }
    } else {
#pragma unroll
      for (int pr = 0; pr < 2; ++pr) {
        const unsigned tx = cur.b[2 * pr].z, ty = cur.b[2 * pr].w;
        cur.b[2 * pr].z = cur.b[2 * pr + 1].x; cur.b[2 * pr].w = cur.b[2 * pr + 1].y;
        cur.b[2 * pr + 1].x = tx; cur.b[2 * pr + 1].y = ty;
      }
#pragma unroll
      for (int nb = 0; nb < 5; ++nb)
#pragma unroll
        for (int m = 0; m < 4; ++m) mfma_ip2(acc[nb][m], cur.a[nb], cur.b[m]);
    }
    SB0();
    const int nx = i + 2;
    if (nx == 6) gl = u2f(__builtin_amdgcn_raw_buffer_load_b32(rws, vo >> 2, G_OFF + rt64 * 256, 16));
    if (nx < 7) { p2_load_b(cur, ra, vo, h0 + nx); p2_load_a(cur, rw, vo, h0 + nx); }
    else if (nx < 10) {
      p2_load_a(cur, rwo, vo, o0 + nx - 7);
      if (nx == 7) lds_wait(L, C_OOK, 1);
      p2_load_b(cur, rao, vo, o0 + nx - 7);
    }
    SB0();
  }
  STAMP_AT(0, 6);
  asm volatile("s_nop 15" ::: "memory");
  lds_wait(L, C_DRAIN, 8);
  SB0();
#pragma unroll
  for (int m = 0; m < 3; ++m)
#pragma unroll
    for (int nb = 0; nb < 5; ++nb) *(LAS f32x4*)(L + ((w * 4 + m) * 5 + nb) * 1024 + vo) = acc[nb][m];
  if (w < 7) {
#pragma unroll
    for (int nb = 0; nb < 5; ++nb) *(LAS f32x4*)(L + ((w * 4 + 3) * 5 + nb) * 1024 + vo) = acc[nb][3];
  }
  SYNCL();
  {
    LANE_VARS(fresh_lane());
    const __amdgpu_buffer_rsrc_t ro_ = mk_rsrc(p.out, S_ * D_ * 2);
    const unsigned op = (unsigned)(((rt64 * 64 + tok) * D_ + ct * 80) * 2);
    if (w < 6) {
      const int m = w % 3, hf = w / 3;
      LAS char* src = L + (m * 5 + hf * 2) * 1024 + vo;
      f32x4 r0 = *(LAS f32x4*)(src), r1 = *(LAS f32x4*)(src + 1024);
#pragma unroll
      for (int sw = 1; sw < 8; ++sw) { r0 += *(LAS f32x4*)(src + sw * 20480); r1 += *(LAS f32x4*)(src + sw * 20480 + 1024); }
      __builtin_amdgcn_raw_buffer_store_b128(cvt8(r0, r1), ro_, op + m * 16 * D_ * 2 + hf * 64 + 16 * g, 0, 16);
    } else if (w == 6) {
#pragma unroll
      for (int m = 0; m < 3; ++m) {
        LAS char* src = L + (m * 5 + 4) * 1024 + vo;
        f32x4 r = *(LAS f32x4*)(src);
#pragma unroll
        for (int sw = 1; sw < 8; ++sw) r += *(LAS f32x4*)(src + sw * 20480);
        __builtin_amdgcn_raw_buffer_store_b64(cvt4(r), ro_, op + m * 16 * D_ * 2 + 128 + 8 * g, 0, 16);
      }
    } else {
#pragma unroll
      for (int sw = 0; sw < 7; ++sw)
#pragma unroll
        for (int nb = 0; nb < 5; ++nb) acc[nb][3] += *(LAS f32x4*)(L + ((sw * 4 + 3) * 5 + nb) * 1024 + vo);
      __builtin_amdgcn_raw_buffer_store_b128(cvt8(acc[0][3], acc[1][3]), ro_, op + 3 * 16 * D_ * 2 + 16 * g, 0, 16);
      __builtin_amdgcn_raw_buffer_store_b128(cvt8(acc[2][3], acc[3][3]), ro_, op + 3 * 16 * D_ * 2 + 64 + 16 * g, 0, 16);
      __builtin_amdgcn_raw_buffer_store_b64(cvt4(acc[4][3]), ro_, op + 3 * 16 * D_ * 2 + 128 + 8 * g, 0, 16);
    }
  }
  STAMP_AT(0, 7);
  STAMP_FLUSH(0);
}

extern "C" void kernel_launch(void* const* d_in, const int* in_sizes, int n_in, void* d_out, int out_size, void* d_ws,
                              size_t ws_size, hipStream_t stream) {
  Args a;
  a.x = (const u16*)d_in[0]; a.wqkv = (const u16*)d_in[1]; a.wphi = (const u16*)d_in[2]; a.wo = (const u16*)d_in[3];
  a.wg = (const u16*)d_in[5]; a.wu = (const u16*)d_in[6]; a.wd = (const u16*)d_in[7]; a.gw = (const u16*)d_in[8];
  a.ws = (char*)d_ws;
  a.out = (u16*)d_out;
  (void)hipFuncSetAttribute((const void*)fused, hipFuncAttributeMaxDynamicSharedMemorySize, K1_LDS);
  fused<<<256, 768, K1_LDS, stream>>>(a);
}
```

```cpp
#include <hip/hip_runtime.h>

typedef unsigned short u16;
typedef unsigned v4u __attribute__((ext_vector_type(4)));
typedef unsigned v2u __attribute__((ext_vector_type(2)));
typedef float f32x4 __attribute__((ext_vector_type(4)));
typedef float f32x2 __attribute__((ext_vector_type(2)));
typedef short s16x4 __attribute__((ext_vector_type(4)));
typedef __bf16 bf16x8 __attribute__((ext_vector_type(8)));
typedef __bf16 bf16x2 __attribute__((ext_vector_type(2)));
#define LAS __attribute__((address_space(3)))
#define GAS __attribute__((address_space(1)))
#define DI __device__ __forceinline__

constexpr int S_ = 2048, D_ = 640, F_ = 1728;
constexpr int NKS = 74, NKH = 54;
constexpr unsigned ACT_OFF = 0, ACT_BYTES = 32u * NKS * 4 * 1024;
constexpr unsigned WT_OFF = ACT_OFF + ACT_BYTES, WT_BYTES = 8u * NKS * 5 * 1024;
constexpr unsigned Q_OFF = WT_OFF + WT_BYTES, Q_BYTES = 32u * 128 * 1024;
constexpr unsigned KVP_REC = 3360;
constexpr unsigned KVP_OFF = Q_OFF + Q_BYTES, KVP_BYTES = 32u * 16 * KVP_REC;
constexpr unsigned G_OFF = KVP_OFF + KVP_BYTES, G_BYTES = S_ * 4;
constexpr unsigned WPF_OFF = G_OFF + G_BYTES, WPF_BYTES = 32u * 2 * 1024;
constexpr unsigned FL_EPOCH = WPF_OFF + WPF_BYTES, FL_ALL = FL_EPOCH + 256, FL_RT = FL_ALL + 2048, FL_AUX = FL_RT + 16 * 128,
                   FL_KV = FL_AUX + 8 * 128, FL_Q = FL_KV + 8 * 128, FL_O = FL_Q + 8 * 256;
constexpr unsigned WS_BYTES = FL_O + 32 * 256;
constexpr unsigned SENT = 0xFFFFFFFFu;

constexpr unsigned L_W = 0, WSLOT = 24576, L_X = 4 * WSLOT, XSLOT = 16384, L_WPHI = L_X + 2 * XSLOT, L_ZERO = L_WPHI + 6400,
                   L_DUMMY = L_WPHI + 8192, K1_LDS = 163840;
constexpr unsigned L_KVS = 161792, L_CTL = 163584;
enum { C_TAG = 0, C_LOOP = 8, C_DRAIN = 12, C_X = 16, C_KV1 = 32, C_KV2 = 36, C_AUXOK = 40, C_PRE = 44, C_CONS = 48, C_OSYNC = 52, C_KVOK = 56, C_T1 = 60, C_HOK = 64, C_T2 = 68, C_OOK = 72 };
constexpr unsigned L_PK = 0, L_V = 8 * 128 * 80;

DI int fresh_lane() {
  int l;
  asm volatile("v_mbcnt_lo_u32_b32 %0, -1, 0\n\tv_mbcnt_hi_u32_b32 %0, -1, %0" : "=v"(l));
  return l;
}
#define LANE_VARS(l) const int lane = (l), g = lane >> 4, q = (lane >> 2) & 3, pp = lane & 3, tok = lane & 15; (void)q; (void)pp; (void)tok; (void)g
DI unsigned rfl(unsigned x) { return __builtin_amdgcn_readfirstlane(x); }
DI unsigned cvtpk(f32x2 v) {
  bf16x2 b = __builtin_convertvector(v, bf16x2);
  return __builtin_bit_cast(unsigned, b);
}
DI unsigned cvtpk(float lo, float hi) { return cvtpk(f32x2{lo, hi}); }
DI v2u cvt4(f32x4 v) { return v2u{cvtpk(v[0], v[1]), cvtpk(v[2], v[3])}; }
DI v4u cvt8(f32x4 a, f32x4 b) { return v4u{cvtpk(a[0], a[1]), cvtpk(a[2], a[3]), cvtpk(b[0], b[1]), cvtpk(b[2], b[3])}; }
DI float bf2f(unsigned b) { return __builtin_bit_cast(float, b << 16); }
DI f32x4 mfma(v4u a, v4u b, f32x4 c) {
  return __builtin_amdgcn_mfma_f32_16x16x32_bf16(__builtin_bit_cast(bf16x8, a), __builtin_bit_cast(bf16x8, b), c, 0, 0, 0);
}
DI v2u tr_read(LAS char* p) {
  s16x4 r = __builtin_amdgcn_ds_read_tr16_b64_v4i16((LAS s16x4*)p);
  return __builtin_bit_cast(v2u, r);
}
DI v4u tr_read2(LAS char* p, int d) {
  v2u a = tr_read(p), b = tr_read(p + d);
  return v4u{a.x, a.y, b.x, b.y};
}
DI __amdgpu_buffer_rsrc_t mk_rsrc(const void* p, unsigned bytes) {
  return __builtin_amdgcn_make_buffer_rsrc((void*)p, (short)0, (int)bytes, 0x00020000);
}
DI float u2f(unsigned u) { return __builtin_bit_cast(float, u); }
DI unsigned f2u(float f) { return __builtin_bit_cast(unsigned, f); }
DI float red4_max(float x) {
  auto r = __builtin_amdgcn_permlane16_swap(f2u(x), f2u(x), false, false);
  unsigned r0 = r[0], r1 = r[1];
  x = fmaxf(u2f(r0), u2f(r1));
  auto t = __builtin_amdgcn_permlane32_swap(f2u(x), f2u(x), false, false);
  r0 = t[0]; r1 = t[1];
  return fmaxf(u2f(r0), u2f(r1));
}
DI float red4_sum(float x) {
  auto r = __builtin_amdgcn_permlane16_swap(f2u(x), f2u(x), false, false);
  unsigned r0 = r[0], r1 = r[1];
  x = u2f(r0) + u2f(r1);
  auto t = __builtin_amdgcn_permlane32_swap(f2u(x), f2u(x), false, false);
  r0 = t[0]; r1 = t[1];
  return u2f(r0) + u2f(r1);
}
DI f32x2 exp2v(f32x2 a) { return f32x2{__builtin_amdgcn_exp2f(a[0]), __builtin_amdgcn_exp2f(a[1])}; }
DI void st16(__amdgpu_buffer_rsrc_t r, unsigned off, v4u v) { __builtin_amdgcn_raw_buffer_store_b128(v, r, off, 0, 16); }
DI void st8(__amdgpu_buffer_rsrc_t r, unsigned off, v2u v) { __builtin_amdgcn_raw_buffer_store_b64(v, r, off, 0, 16); }
#define AUX_DELAY 3
#define PF_MODE 1
#define PF_D0 8
#define PF_D1 1
#define SB0() __builtin_amdgcn_sched_barrier(0)
#define SYNC(n) do { SB0(); asm volatile("s_waitcnt vmcnt(" #n ") lgkmcnt(0)\n\ts_barrier" ::: "memory"); SB0(); } while (0)
#define NT_A "nt"
#define LDSDMA(m0v, vo, rs, so) \
  asm volatile("s_mov_b32 m0, %0\n\ts_nop 0\n\tbuffer_load_dwordx4 %1, %2, %3 offen " NT_A " lds" :: "s"(m0v), "v"(vo), "s"(rs), "s"(so) : "memory")

#define STAMP_DECL
#define STAMP_AT(kern, i)
#define STAMP_FLUSH(kern)
#define STAMP_ARG
#define STAMP_PASS

struct Args {
  const u16 *x, *wqkv, *wphi, *wo, *wg, *wu, *wd, *gw;
  char* ws;
  u16* out;
};

#define SYNCL() do { SB0(); asm volatile("s_waitcnt lgkmcnt(0)\n\ts_barrier" ::: "memory"); SB0(); } while (0)
#define NT_D ""
#define NT_P ""
#define DMA(m0v, vo, rs, so, off) \
  asm volatile("s_mov_b32 m0, %0\n\ts_nop 0\n\tbuffer_load_dwordx4 %1, %2, %3 offen offset:%4 " NT_D " lds" :: "s"(m0v), "v"(vo), "s"(rs), "s"(so), "i"(off) : "memory")
#define DMAS(m0v, vo, rs, so, off) \
  asm volatile("s_mov_b32 m0, %0\n\ts_nop 0\n\tbuffer_load_dwordx4 %1, %2, %3 offen offset:%4 sc1 lds" :: "s"(m0v), "v"(vo), "s"(rs), "s"(so), "i"(off) : "memory")

template <int MODE> struct MI { static constexpr int NB = MODE == 1 ? 5 : 6; };
template <int MODE, int B, int R, int SLOT> DI v2u a_read(LAS char* L, unsigned (&aoff)[6]) {
  constexpr int I = MODE == 0 ? B >> 1 : B;
  if constexpr (MODE == 2 && B == 5) {
    const v2u t = *(LAS v2u*)(L + aoff[5] + SLOT * 64 + R * 8);
    if constexpr (R == 1 && SLOT == 3) asm volatile("v_add_u32 %0, 0x100, %0" : "+v"(aoff[5]));
    return t;
  }
  const v2u t = tr_read(L + L_W + (SLOT & 1) * WSLOT + aoff[I] + ((MODE == 0 && (B & 1)) ? 12288 : 0) + R * 1536);
  if constexpr (R == 1 && (MODE != 0 || (B & 1))) {
    if constexpr (SLOT == 1) asm volatile("v_add_u32 %0, 0xc000, %0" : "+v"(aoff[I]));
    if constexpr (SLOT == 3) asm volatile("v_subrev_u32 %0, 0xc000, %0" : "+v"(aoff[I]));
  }
  return t;
}
DI void mfma_ip(f32x4& c, const v4u& a, const v4u& b) {
  asm volatile("v_mfma_f32_16x16x32_bf16 %0, %1, %2, %0" : "+v"(c) : "v"(a), "v"(b));
}
template <int MODE, int T, int J = 0>
DI void k1_step(f32x4 (&acc)[4][MI<MODE>::NB], v4u (&a)[4], v4u (&x)[2][4], LAS char* L, unsigned (&aoff)[6],
                const unsigned (&xoff)[2]) {
  constexpr int NB = MI<MODE>::NB, R0 = T * NB;
  if constexpr (J < 4 * NB) {
    constexpr int b = J >> 2, r = J & 3, nb = b + 2;
    if constexpr (r < 2) {
      v2u t = a_read<MODE, nb % NB, r, (nb < NB ? T : T + 1) & 3>(L, aoff);
      if constexpr (r == 0) { a[(R0 + nb) & 3].x = t.x; a[(R0 + nb) & 3].y = t.y; }
      else { a[(R0 + nb) & 3].z = t.x; a[(R0 + nb) & 3].w = t.y; }
    } else if constexpr (r == 2 && b < 4)
      x[(T + 1) & 1][b] = *(LAS v4u*)(L + xoff[(T + 1) & 1] + ((((T + 1) >> 1) & 1) * XSLOT + b * 2048));
    mfma_ip(acc[r][b], a[(R0 + b) & 3], x[T & 1][r]);
    SB0();
    k1_step<MODE, T, J + 1>(acc, a, x, L, aoff, xoff);
  }
}
template <int MODE>
DI void k1_mainloop(f32x4 (&acc)[4][MI<MODE>::NB], LAS char* L, unsigned (&aoff)[6], unsigned xoff0 STAMP_ARG) {
  unsigned xoff[2] = {xoff0 + L_X, (xoff0 ^ 64) + L_X};
  asm volatile("" : "+v"(xoff[0]), "+v"(xoff[1]));
  v4u a[4], x[2][4];
#pragma unroll
  for (int m = 0; m < 4; ++m)
#pragma unroll
    for (int b = 0; b < MI<MODE>::NB; ++b) acc[m][b] = f32x4{0.f, 0.f, 0.f, 0.f};
  SYNCL();
  STAMP_AT(0, 4);
#pragma unroll
  for (int m = 0; m < 4; ++m) x[0][m] = *(LAS v4u*)(L + xoff[0] + m * 2048);
  {
    const v2u t0 = a_read<MODE, 0, 0, 0>(L, aoff), t1 = a_read<MODE, 0, 1, 0>(L, aoff);
    const v2u t2 = a_read<MODE, 1, 0, 0>(L, aoff), t3 = a_read<MODE, 1, 1, 0>(L, aoff);
    a[0] = v4u{t0.x, t0.y, t1.x, t1.y}; a[1] = v4u{t2.x, t2.y, t3.x, t3.y};
  }
#pragma unroll 1
  for (int i = 0; i < 20; i += 4) {
    SYNCL(); k1_step<MODE, 0>(acc, a, x, L, aoff, xoff);
    SYNCL(); k1_step<MODE, 1>(acc, a, x, L, aoff, xoff);
    SYNCL(); k1_step<MODE, 2>(acc, a, x, L, aoff, xoff);
    SYNCL(); k1_step<MODE, 3>(acc, a, x, L, aoff, xoff);
  }
  asm volatile("s_nop 15" ::: "memory");
  __builtin_amdgcn_s_setprio(2);
  SB0();
}

DI unsigned gelu_mul2(f32x2 x, f32x2 u) {
  const float k0 = -2.0f * 0.7978845608028654f * 1.4426950408889634f, k1 = k0 * 0.044715f;
  f32x2 t = x * x;
  f32x2 a = x * (t * k1 + k0);
  f32x2 d = exp2v(a) + 1.0f;
  f32x2 r = f32x2{__builtin_amdgcn_rcpf(d[0]), __builtin_amdgcn_rcpf(d[1])};
  return cvtpk((x * u) * r);
}
DI v2u gelu_mul4(f32x4 x, f32x4 u) {
  return v2u{gelu_mul2(f32x2{x[0], x[1]}, f32x2{u[0], u[1]}), gelu_mul2(f32x2{x[2], x[3]}, f32x2{u[2], u[3]})};
}

#define OPRIO 3
#define CODE_PF 49152
#define POLL_SLEEP 6
DI void lds_arrive(LAS char* L, unsigned word) {
  asm volatile("s_waitcnt lgkmcnt(0)" ::: "memory");
  if (fresh_lane() == 0) __hip_atomic_fetch_add((LAS unsigned*)(L + L_CTL + word), 1u, __ATOMIC_RELAXED, __HIP_MEMORY_SCOPE_WORKGROUP);
}
DI void lds_wait(LAS char* L, unsigned word, unsigned target) {
  volatile LAS unsigned* f = (volatile LAS unsigned*)(L + L_CTL + word);
  for (unsigned spins = 0; rfl(*f) < target && spins < (1u << 22); ++spins) __builtin_amdgcn_s_sleep(1);
  asm volatile("" ::: "memory");
}
DI void poll_line(__amdgpu_buffer_rsrc_t rws, unsigned line, unsigned mask, v2u tag) {
  const int l = fresh_lane() & 31;
  const bool need = (mask >> l) & 1u;
  for (unsigned spins = 0; spins < (1u << 18); ++spins) {
    const v2u v = __builtin_amdgcn_raw_buffer_load_b64(rws, l * 8, line, 16);
    if (!__any(need && (v.x != tag.x || v.y != tag.y))) break;
    __builtin_amdgcn_s_sleep(POLL_SLEEP);
  }
  __builtin_amdgcn_fence(__ATOMIC_ACQUIRE, "wavefront");
}

DI void poll_once(LAS char* L, unsigned ticket, unsigned okw, __amdgpu_buffer_rsrc_t rws, unsigned line, unsigned mask, v2u tag) {
  unsigned t = 0;
  if (fresh_lane() == 0) t = __hip_atomic_fetch_add((LAS unsigned*)(L + L_CTL + ticket), 1u, __ATOMIC_RELAXED, __HIP_MEMORY_SCOPE_WORKGROUP);
  if (rfl(t) == 0) { poll_line(rws, line, mask, tag); lds_arrive(L, okw); }
  else lds_wait(L, okw, 1);
}
DI void mfma_ip2(f32x4& c, const v4u& a, const v4u& b) {
  asm volatile("s_nop 1\n\tv_mfma_f32_16x16x32_bf16 %0, %1, %2, %0" : "+v"(c) : "v"(a), "v"(b));
}
DI void mfma_init(f32x4& c, const v4u& a, const v4u& b) {
  asm volatile("s_nop 1\n\tv_mfma_f32_16x16x32_bf16 %0, %1, %2, 0" : "=&v"(c) : "v"(a), "v"(b));
}
struct Stage { v4u a[5], b[4]; };
DI void p2_load_b(Stage& st, __amdgpu_buffer_rsrc_t ra, unsigned vo, int ks) {
#pragma unroll
  for (int i = 0; i < 4; ++i) st.b[i] = __builtin_amdgcn_raw_buffer_load_b128(ra, vo + i * 1024, ks * 4096, 16);
}
DI void p2_load_a(Stage& st, __amdgpu_buffer_rsrc_t rw, unsigned vo, int ks) {
#pragma unroll
  for (int nb = 0; nb < 5; ++nb) st.a[nb] = __builtin_amdgcn_raw_buffer_load_b128(rw, vo + nb * 1024, ks * 5120, 16);
}

__global__ __launch_bounds__(768, 3) void fused(Args p) {
  extern __shared__ __attribute__((aligned(16))) char smem[];
  LAS char* L = (LAS char*)smem;
  const int w = rfl(threadIdx.x >> 6);
  const int bid = blockIdx.x, xcd = bid & 7, idx = bid >> 3;
  const unsigned long long CSMAP = 0xDC87BA65E943F210ull;
  const int rt = (xcd & 1) * 8 + (idx & 7), cs = (int)(CSMAP >> (4 * ((xcd >> 1) * 4 + (idx >> 3)))) & 15;
  char* ws = p.ws;
  const __amdgpu_buffer_rsrc_t rws = mk_rsrc(ws, WS_BYTES);
  STAMP_DECL;
  STAMP_AT(0, 0);
  const bool isAUX = cs == 15, isGU = cs < 9, isKV = cs >= 11 && cs < 15;

  if (w == 1) {
    unsigned long long pc;
    asm volatile("s_getpc_b64 %0" : "=s"(pc));
    const __amdgpu_buffer_rsrc_t rc = mk_rsrc((const void*)(pc & ~255ull), CODE_PF);
    DMA(L_DUMMY + 4096, (unsigned)(idx * 2048 + fresh_lane() * 16), rc, 0, 0);
    DMA(L_DUMMY + 4096, (unsigned)(idx * 2048 + fresh_lane() * 16), rc, 0, 1024);
  }
  if (w == 0) {
    const int lane = fresh_lane();
    const v2u e = __builtin_amdgcn_raw_buffer_load_b64(rws, 0, FL_EPOCH, 16);
    unsigned long long t = (((unsigned long long)e.y << 32) | e.x) * 0x9E3779B97F4A7C15ull + 0xD1B54A32D192ED03ull;
    t ^= t >> 29;
    if (lane < 32) *(LAS unsigned*)(L + L_CTL + lane * 4) = lane == 0 ? (unsigned)t : lane == 1 ? (unsigned)(t >> 32) : 0u;
  }

  if (isAUX) {
    LANE_VARS(fresh_lane());
    if (w >= 8) {
      SYNCL();
    } else {
      const __amdgpu_buffer_rsrc_t rd = mk_rsrc(p.wd, F_ * D_ * 2), ro = mk_rsrc(p.wo, D_ * D_ * 2);
      unsigned vlo[5];
#pragma unroll
      for (int j = 0; j < 5; ++j) {
        const int Lc = ((w & 3) * 5 + j) * 64 + lane;
        vlo[j] = (unsigned)((Lc / 80) * 1280 + (Lc % 80) * 16);
      }
      auto issue = [&](int sl) {
        const int s = rt + 16 * sl;
        int b0, b1;
        if (s < NKH) {
          const int u = s / 6, e = s % 6;
          if (e < 4) { b0 = 192 * u + 48 * e; b1 = b0 + 16; } else { b0 = 192 * u + 96 * (e - 4) + 32; b1 = b0 + 48; }
        } else { b0 = 32 * (s - NKH); b1 = b0 + 16; }
        const __amdgpu_buffer_rsrc_t rs = s < NKH ? rd : ro;
        const unsigned so = (unsigned)((w < 4 ? b0 : b1) * 1280), m0 = (unsigned)((sl % 3) * 40960 + w * 5120);
#pragma unroll
        for (int j = 0; j < 5; ++j) LDSDMA(m0 + j * 1024, vlo[j], rs, so);
      };
      auto retile = [&](int sl) {
        const int s = rt + 16 * sl;
        LAS char* base = L + (sl % 3) * 40960 + (4 * g + q) * 1280 + w * 160;
        const unsigned dst = s < NKS ? WT_OFF + (w * NKS + s) * 5120 + lane * 16 : 0x80000000u;
#pragma unroll
        for (int nb = 0; nb < 5; ++nb) {
          const int col = nb < 4 ? 32 * (nb >> 1) + 8 * pp + 4 * (nb & 1) : 64 + 4 * pp;
          st16(rws, dst + nb * 1024, tr_read2(base + col * 2, 16 * 1280));
        }
      };
      SYNCL();
      for (int i = 0; i < AUX_DELAY; ++i) __builtin_amdgcn_s_sleep(16);
#define AUX_SYNC(n, k) do { asm volatile("s_waitcnt vmcnt(" #n ")" ::: "memory"); lds_arrive(L, C_KV1); lds_wait(L, C_KV1, 8 * (k)); } while (0)
      issue(0); issue(1); issue(2);
      AUX_SYNC(10, 1); retile(0);
      AUX_SYNC(10, 2); issue(3); retile(1);
      AUX_SYNC(15, 3); issue(4); retile(2);
      AUX_SYNC(15, 4); retile(3);
      AUX_SYNC(10, 5); retile(4);
    }
  } else {
    const __amdgpu_buffer_rsrc_t rx = mk_rsrc(p.x + rt * 128 * D_, (unsigned)((S_ - rt * 128) * D_ * 2));
    const unsigned pitch = isGU ? F_ * 2 : 3 * D_ * 2, total = D_ * pitch;
    unsigned boffA, boffB;
    if (isGU) boffA = boffB = 384u * cs;
    else if (!isKV) { boffA = 640u * (cs - 9); boffB = boffA + 256u; }
    else { const int j = cs - 11; boffA = 1280u + 320u * j - ((j & 1) ? 64u : 0u); boffB = boffA + 1280u; }
    const char* baseA = (const char*)(isGU ? p.wg : p.wqkv) + boffA;
    const char* baseB = (const char*)(isGU ? p.wu : p.wqkv) + boffB;
    const int mh = w & 1, n = w >> 1;
    const int rt64 = rt * 2 + mh;
    if (w >= 8) {
      __builtin_amdgcn_s_setprio(3);
      const int lane = fresh_lane();
      const int lw = w - 8, sub = lw >> 1;
      const int r0 = 32 * lw + (lane >> 3);
      const unsigned vx0 = (unsigned)(r0 * 1280 + (((lane & 7) ^ ((r0 >> 1) & 7)) * 16)), vx1 = (vx0 ^ 64u) + 8 * 1280 - 1024;
      const unsigned m0x = L_X + lw * 4096;
#define LOAD_X(stage, slot, e) do { const unsigned m0_ = m0x + (unsigned)(slot) * XSLOT + (e) * 2048, so_ = (unsigned)(stage) * 128 + (e) * 16 * 1280; \
      DMA(m0_, vx0, rx, so_, 0); DMA(m0_, vx1, rx, so_, 1024); } while (0)
#define LOAD_XX(stage) do { LOAD_X(stage, (stage) & 1, 0); LOAD_X(stage, (stage) & 1, 1); } while (0)
      if (cs == 9 && lw < 2) {
        const __amdgpu_buffer_rsrc_t rg = mk_rsrc(p.gw, D_ * 2);
        DMA(L_WPHI + lw * 1024, (unsigned)(lw * 1024 + lane * 16), rg, 0, 0);
      }
      LOAD_XX(0);
      const __amdgpu_buffer_rsrc_t rw = mk_rsrc((sub ? baseB : baseA) + (lw & 1) * 16 * pitch, total - (sub ? boffB : boffA) - (lw & 1) * 16 * pitch);
      unsigned vw[6];
#pragma unroll
      for (int j = 0; j < 3; ++j) {
        const int Lc = 64 * j + lane, kr = Lc / 24, pos = Lc % 24;
        const unsigned c0 = (unsigned)((pos >> 3) * 128 + (((pos & 7) ^ (kr & 2)) * 16));
        vw[j] = (unsigned)kr * pitch + c0 - 1024u * j;
        vw[3 + j] = (unsigned)(kr + 8) * pitch + (c0 ^ 64u) - 1024u * j;
      }
      const unsigned m0w = L_W + sub * 12288 + (lw & 1) * 6144, wstep = 32 * pitch;
#define LOAD_W(s) do { const unsigned m0_ = m0w + (unsigned)((s) & 3) * WSLOT, so_ = (unsigned)(s) * wstep; \
      DMA(m0_, vw[0], rw, so_, 0); DMA(m0_, vw[1], rw, so_, 1024); DMA(m0_, vw[2], rw, so_, 2048); \
      DMA(m0_ + 3072, vw[3], rw, so_, 0); DMA(m0_ + 3072, vw[4], rw, so_, 1024); DMA(m0_ + 3072, vw[5], rw, so_, 2048); } while (0)
      const __amdgpu_buffer_rsrc_t rpf = lw == 3 ? rx : mk_rsrc(((rt & 1) ? baseB : baseA) + 8 * (rt >> 1) * pitch, total - ((rt & 1) ? boffB : boffA) - 8 * (rt >> 1) * pitch);
      const unsigned vpf = lw == 3 ? (unsigned)((8 * cs + (lane >> 3)) * 1280 + (lane & 7) * 16) : lw == 0 ? vw[0] : lw == 1 ? vw[1] + 1024u : vw[2] + 2048u;
      const unsigned pfstep = lw == 3 ? 128u : 64u * pitch;
#define PF(stage) DMA(L_DUMMY + lw * 1024, vpf, rpf, (unsigned)(stage) * pfstep, 0)
      LOAD_W(0);
      LOAD_W(1); LOAD_XX(1);
      LOAD_W(2); PF(2); PF(3);
      SYNC(18);
      SYNC(8); LOAD_W(3); PF(4);
      SYNC(9); LOAD_XX(2); LOAD_W(4); PF(5);
      SYNC(12); LOAD_W(5); PF(6);
      SYNC(8); LOAD_XX(3); LOAD_W(6); PF(7);
      SYNC(12); LOAD_W(7); PF(8);
      SYNC(8); LOAD_XX(4); LOAD_W(8); PF(9);
      SYNC(12); LOAD_W(9);
      SYNC(7); LOAD_XX(5); LOAD_W(10);
      SYNC(10); LOAD_W(11);
#pragma unroll 1
      for (int i = 9; i < 15; i += 2) {
        SYNC(6); LOAD_X((i + 3) >> 1, ((i + 3) >> 1) & 1, 0); LOAD_X((i + 3) >> 1, ((i + 3) >> 1) & 1, 1); LOAD_W(i + 3);
        SYNC(10); LOAD_W(i + 4);
      }
      SYNC(6); LOAD_XX(9); LOAD_W(18);
      SYNC(10); LOAD_W(19);
      if (isKV) {
        const __amdgpu_buffer_rsrc_t rp = mk_rsrc(p.wphi + 8 * (cs - 11) * 400, 6400);
        DMA(L_WPHI + lw * 2048, (unsigned)(lw * 2048 + lane * 16), rp, 0, 0);
        DMA(L_WPHI + lw * 2048, (unsigned)(lw * 2048 + lane * 16), rp, 0, 1024);
      }
      SYNC(6);
      SYNC(0);
      SYNC(0);
    } else if (isGU) {
#define FRAG_VARS LANE_VARS(fresh_lane()); const int sw = ((q >> 1) & 1) | ((g & 1) << 1); \
  const unsigned xoff0 = (unsigned)((64 * mh + tok) * 128 + ((g ^ (tok >> 1)) * 16))
      f32x4 acc[4][6];
      {
        FRAG_VARS;
        unsigned aoff[6];
#pragma unroll
        for (int b = 0; b < 3; ++b) {
          const int ch = 6 * n + 2 * b + (pp >> 1);
          aoff[b] = (unsigned)((8 * g + q) * 384 + (ch >> 3) * 128 + (((ch & 7) ^ (2 * sw)) * 16) + (pp & 1) * 8);
        }
        aoff[3] = aoff[4] = aoff[5] = 0;
        k1_mainloop<0>(acc, L, aoff, xoff0 STAMP_PASS);
      }
      STAMP_AT(0, 1);
      const int lane = fresh_lane();
      const unsigned actA = ACT_OFF + ((rt64 * NKS + 6 * cs + n) * 4) * 1024 + lane * 16;
      const unsigned actB = ACT_OFF + ((rt64 * NKS + 6 * cs + 4 + (n >> 1)) * 4 + 2 * (n & 1)) * 1024 + lane * 16;
      v2u h2[4];
#pragma unroll
      for (int m = 0; m < 4; ++m) h2[m] = gelu_mul4(acc[m][4], acc[m][5]);
      const bool odd = n & 1;
      *(LAS v4u*)(L + L_WPHI + w * 1024 + lane * 16) = odd ? v4u{h2[0].x, h2[0].y, h2[1].x, h2[1].y} : v4u{h2[2].x, h2[2].y, h2[3].x, h2[3].y};
      const unsigned xw = C_X + 4 * ((w & 1) | ((w >> 2) << 1));
      lds_arrive(L, xw);
      const v2u k0 = odd ? h2[2] : h2[0], k1 = odd ? h2[3] : h2[1];
#pragma unroll
      for (int m = 0; m < 4; ++m) {
        const v2u h0 = gelu_mul4(acc[m][0], acc[m][1]), h1 = gelu_mul4(acc[m][2], acc[m][3]);
        st16(rws, actA + m * 1024, v4u{h0.x, h0.y, h1.x, h1.y});
        if (m == 2) {
          lds_wait(L, xw, 2);
          const v4u o = *(LAS v4u*)(L + L_WPHI + (w ^ 2) * 1024 + lane * 16);
          st16(rws, actB, odd ? v4u{o.x, o.y, k0.x, k0.y} : v4u{k0.x, k0.y, o.x, o.y});
          st16(rws, actB + 1024, odd ? v4u{o.z, o.w, k1.x, k1.y} : v4u{k1.x, k1.y, o.z, o.w});
        }
      }
    } else {
#define QKV_AOFF \
    FRAG_VARS; \
    unsigned aoff[6]; \
    { \
      aoff[5] = L_WPHI + g * 16; \
      const int offch = isKV ? ((cs - 11) & 1) * 4 : (n >> 1) * 4; \
      _Pragma("unroll") for (int b = 0; b < 5; ++b) { \
        const int u = 2 * offch + 20 * (n & 1) + 5 * pp + b, ch = u >> 1; \
        aoff[b] = (unsigned)((n >> 1) * 12288 + (8 * g + q) * 384 + (ch >> 3) * 128 + (((ch & 7) ^ (2 * sw)) * 16) + (u & 1) * 8); \
      } \
    }
      v4u c01[4], c23[4];
      v2u c4[4];
      if (cs == 9) {
        f32x4 acc[4][6];
        {
          QKV_AOFF
          k1_mainloop<2>(acc, L, aoff, xoff0 STAMP_PASS);
        }
#pragma unroll
        for (int m = 0; m < 4; ++m) { c01[m] = cvt8(acc[m][0], acc[m][1]); c23[m] = cvt8(acc[m][2], acc[m][3]); c4[m] = cvt4(acc[m][4]); }
        if (n == 0) {
          LANE_VARS(fresh_lane());
          const float a = g == 0 ? acc[0][5][0] : g == 1 ? acc[1][5][0] : g == 2 ? acc[2][5][0] : acc[3][5][0];
          __builtin_amdgcn_raw_buffer_store_b32(f2u(1.0f / (1.0f + __expf(-a))), rws, G_OFF + (rt64 * 64 + lane) * 4, 0, 16);
        }
      } else {
        f32x4 acc[4][5];
        {
          QKV_AOFF
          k1_mainloop<1>(acc, L, aoff, xoff0 STAMP_PASS);
        }
#pragma unroll
        for (int m = 0; m < 4; ++m) { c01[m] = cvt8(acc[m][0], acc[m][1]); c23[m] = cvt8(acc[m][2], acc[m][3]); c4[m] = cvt4(acc[m][4]); }
      }
      STAMP_AT(0, 1);
      LANE_VARS(fresh_lane());
      if (!isKV) {
          const int head = 16 * (cs - 9) + 4 * n + g;
        const unsigned qp = Q_OFF + (head * 128 + rt64 * 4) * 1024 + tok * 16;
#pragma unroll
        for (int m = 0; m < 4; ++m) {
          st16(rws, qp + m * 1024, v4u{c01[m].x, c01[m].y, c4[m].x & 0xffffu, 0u});
          st16(rws, qp + m * 1024 + 256, v4u{c01[m].z, c01[m].w, c4[m].x >> 16, 0u});
          st16(rws, qp + m * 1024 + 512, v4u{c23[m].x, c23[m].y, c4[m].y & 0xffffu, 0u});
          st16(rws, qp + m * 1024 + 768, v4u{c23[m].z, c23[m].w, c4[m].y >> 16, 0u});
        }
      } else {
        const int hl = 4 * (n & 1) + g;
        LAS char* xch = L + L_DUMMY + (w & 3) * 5120;
        if (n < 2) {
#pragma unroll
          for (int mm = 0; mm < 2; ++mm) {
            *(LAS v4u*)(xch + mm * 2560 + lane * 16) = c01[2 + mm];
            *(LAS v4u*)(xch + mm * 2560 + 1024 + lane * 16) = c23[2 + mm];
            *(LAS v2u*)(xch + mm * 2560 + 2048 + lane * 8) = c4[2 + mm];
          }
        }
        lds_arrive(L, C_KV1);
        v4u wf[5][3];
        {
          LAS char* wb = L + (pp == g ? L_WPHI + (unsigned)(hl * 800 + q * 40) : L_ZERO);
#pragma unroll
          for (int bo = 0; bo < 5; ++bo)
#pragma unroll
            for (int s3 = 0; s3 < 3; ++s3) {
              v2u lo = tr_read(wb + s3 * 320 + bo * 8), hi = v2u{0u, 0u};
              if (s3 < 2) hi = tr_read(wb + s3 * 320 + 160 + bo * 8);
              wf[bo][s3] = v4u{lo.x, lo.y, hi.x, hi.y};
            }
        }
        lds_wait(L, C_KV1, 8);
        if (n >= 2) {
#pragma unroll
          for (int m = 0; m < 4; ++m) {
            LAS char* dst = L + L_V + (hl * 128 + 64 * mh + 16 * m + tok) * 48;
            *(LAS v4u*)(dst) = c01[m];
            *(LAS v4u*)(dst + 16) = c23[m];
            *(LAS v4u*)(dst + 32) = v4u{c4[m].x, c4[m].y, 0x00003f80u, 0u};
          }
#pragma unroll
          for (int mm = 0; mm < 2; ++mm) {
            c01[mm] = *(LAS v4u*)(xch + mm * 2560 + lane * 16);
            c23[mm] = *(LAS v4u*)(xch + mm * 2560 + 1024 + lane * 16);
            c4[mm] = *(LAS v2u*)(xch + mm * 2560 + 2048 + lane * 8);
          }
        }
        {
          LAS char* dst0 = L + L_PK + (hl * 128 + 64 * mh + (n >= 2 ? 32 : 0) + tok) * 80;
          f32x4 pk[2][5];
#pragma unroll
          for (int m = 0; m < 2; ++m) {
            const v4u kf[3] = {c01[m], c23[m], v4u{c4[m].x, c4[m].y, 0u, 0u}};
#pragma unroll
            for (int bo = 0; bo < 5; ++bo) {
              pk[m][bo] = f32x4{0.f, 0.f, 0.f, 0.f};
#pragma unroll
              for (int s3 = 0; s3 < 3; ++s3) pk[m][bo] = mfma(wf[bo][s3], kf[s3], pk[m][bo]);
            }
          }
#pragma unroll
          for (int m = 0; m < 2; ++m) {
            float mx = pk[m][0][0], mn = pk[m][0][0];
#pragma unroll
            for (int bo = 0; bo < 5; ++bo)
#pragma unroll
              for (int r = 0; r < 4; ++r) { mx = fmaxf(mx, pk[m][bo][r]); mn = fminf(mn, pk[m][bo][r]); }
            const float l2e = 1.4426950408889634f;
            const float mxs = -mx * l2e, mns = mn * l2e;
            f32x2 ep[10], en[10];
#pragma unroll
            for (int i = 0; i < 10; ++i) {
              const f32x2 v = f32x2{pk[m][i >> 1][2 * (i & 1)], pk[m][i >> 1][2 * (i & 1) + 1]};
              ep[i] = v * l2e + mxs;
              en[i] = v * -l2e + mns;
            }
#pragma unroll
            for (int i = 0; i < 10; ++i) { ep[i] = exp2v(ep[i]); en[i] = exp2v(en[i]); }
            const f32x2 sp = (((ep[0] + ep[1]) + (ep[2] + ep[3])) + ((ep[4] + ep[5]) + (ep[6] + ep[7]))) + (ep[8] + ep[9]);
            const f32x2 sn = (((en[0] + en[1]) + (en[2] + en[3])) + ((en[4] + en[5]) + (en[6] + en[7]))) + (en[8] + en[9]);
            const float rp = __builtin_amdgcn_rcpf(sp[0] + sp[1]), rn = __builtin_amdgcn_rcpf(sn[0] + sn[1]);
            unsigned o[20];
#pragma unroll
            for (int i = 0; i < 10; ++i) { o[i] = cvtpk(ep[i] * rp); o[10 + i] = cvtpk(en[i] * rn); }
            LAS char* dst = dst0 + m * 16 * 80;
#pragma unroll
            for (int i = 0; i < 5; ++i) *(LAS v4u*)(dst + i * 16) = v4u{o[4 * i], o[4 * i + 1], o[4 * i + 2], o[4 * i + 3]};
          }
        }
        lds_arrive(L, C_KV2);
        lds_wait(L, C_KV2, 8);
        {
          f32x4 kv[3][2];
#pragma unroll
          for (int fb = 0; fb < 3; ++fb) { kv[fb][0] = f32x4{0.f, 0.f, 0.f, 0.f}; kv[fb][1] = f32x4{0.f, 0.f, 0.f, 0.f}; }
          LAS char* pa = L + L_PK + (w * 128 + 8 * g + q) * 80 + pp * 8;
          LAS char* pb = L + L_V + (w * 128 + 8 * g + q) * 48 + pp * 8;
#pragma unroll
          for (int ks = 0; ks < 4; ++ks) {
            v4u af[3], bf[2];
#pragma unroll
            for (int fb = 0; fb < 3; ++fb) af[fb] = tr_read2(pa + ks * 32 * 80 + fb * 32, 4 * 80);
#pragma unroll
            for (int vb = 0; vb < 2; ++vb) bf[vb] = tr_read2(pb + ks * 32 * 48 + vb * 32, 4 * 48);
#pragma unroll
            for (int fb = 0; fb < 3; ++fb)
#pragma unroll
              for (int vb = 0; vb < 2; ++vb) kv[fb][vb] = mfma(af[fb], bf[vb], kv[fb][vb]);
          }
          const int head = 8 * (cs - 11) + w;
          const unsigned rec = KVP_OFF + (head * 16 + rt) * KVP_REC;
          const unsigned o4 = rec + tok * 64 + g * 16, o2 = g < 2 ? rec + tok * 32 + g * 16 : 0x80000000u;
          const unsigned t5 = tok < 5 ? 0u : 0x80000000u;
          st16(rws, o4, __builtin_bit_cast(v4u, kv[0][0]));
          st16(rws, o4 + 1024, __builtin_bit_cast(v4u, kv[1][0]));
          st16(rws, o2 + 2048, __builtin_bit_cast(v4u, kv[2][0]));
          st16(rws, (o4 | t5) + 2560, __builtin_bit_cast(v4u, kv[0][1]));
          st16(rws, (o4 | t5) + 2880, __builtin_bit_cast(v4u, kv[1][1]));
          st16(rws, (o2 | t5) + 3200, __builtin_bit_cast(v4u, kv[2][1]));
            }
      }
    }
  }
  STAMP_AT(0, 2);

  const v2u tagv = *(LAS v2u*)(L + L_CTL + C_TAG);
  const v2u tag = v2u{rfl(tagv.x), rfl(tagv.y)};
  const int rt64 = (xcd >> 1) * 8 + (idx & 7), ct = (xcd & 1) * 4 + (idx >> 3);
  const __amdgpu_buffer_rsrc_t rw = mk_rsrc(ws + WT_OFF + ct * (NKS * 5120), NKH * 5120);
  const f32x4 z4 = {0.f, 0.f, 0.f, 0.f};

  if (w < 8) {
    asm volatile("s_waitcnt vmcnt(0) lgkmcnt(0)" ::: "memory");
    const int lane = fresh_lane();
    unsigned old = 0;
    if (lane == 0) old = __hip_atomic_fetch_add((LAS unsigned*)(L + L_CTL + C_DRAIN), 1u, __ATOMIC_RELAXED, __HIP_MEMORY_SCOPE_WORKGROUP);
    if (rfl(old) == 7u) {
      unsigned off = 0x80000000u;
      if (lane == 0) off = FL_ALL + bid * 8;
      else if (isGU) { if (lane == 1) off = FL_RT + rt * 128 + cs * 8; }
      else if (isAUX) { if (lane <= 8) off = FL_AUX + (lane - 1) * 128 + rt * 8; }
      else if (isKV) { if (lane <= 2) off = FL_KV + ((cs - 11) * 2 + lane - 1) * 128 + rt * 8; }
      else if (lane <= 8) off = FL_Q + (lane - 1) * 256 + (rt * 2 + cs - 9) * 8;
      else if (lane == 9) off = FL_RT + rt * 128 + cs * 8;
      st8(rws, off, tag);
    }
  }

  if (w >= 8) {
    LANE_VARS(fresh_lane());
    const unsigned vo = lane * 16;
    const int lw = w - 8;
    __builtin_amdgcn_s_setprio(1);
    const int head = xcd * 4 + (idx & 3), rg = idx >> 2, t = lw * 64 + lane;
    v4u wa0, wa1;
    {
      const u16* wp = p.wphi + head * 400;
      unsigned e0[5], e1[5];
#pragma unroll
      for (int j = 0; j < 5; ++j) {
        const int d = j < 4 ? 4 * g + j : 16 + g;
        e0[j] = wp[d * 20 + tok];
        e1[j] = wp[d * 20 + 16 + (tok & 3)];
      }
      wa0 = v4u{e0[0] | e0[1] << 16, e0[2] | e0[3] << 16, e0[4], 0u};
      wa1 = v4u{e1[0] | e1[1] << 16, e1[2] | e1[3] << 16, e1[4], 0u};
    }
    if (isAUX) lds_wait(L, C_DRAIN, 8);
    if (lw == 1) { poll_line(rws, FL_Q + xcd * 256, (0x5u << (4 * rg + (head >> 4))) | (0x5u << (4 * rg)), tag); lds_arrive(L, C_T1); }
    else lds_wait(L, C_T1, 1);
    STAMP_AT(1, 3);
    v4u qf[4];
#pragma unroll
    for (int m = 0; m < 4; ++m)
      qf[m] = __builtin_amdgcn_raw_buffer_load_b128(rws, vo + m * 1024, Q_OFF + (head * 128 + rg * 16 + lw * 4) * 1024, 16);
    const float gl = u2f(__builtin_amdgcn_raw_buffer_load_b32(rws, lane * 4, G_OFF + (rg * 256 + lw * 64) * 4, 16));
    v4u fp[4], fn[4];
#pragma unroll
    for (int m = 0; m < 4; ++m) {
      const f32x4 p0 = mfma(wa0, qf[m], z4);
      f32x4 p1 = mfma(wa1, qf[m], z4);
      if (g != 0) p1 = p0;
      float mx = fmaxf(fmaxf(fmaxf(p0[0], p0[1]), fmaxf(p0[2], p0[3])), fmaxf(fmaxf(p1[0], p1[1]), fmaxf(p1[2], p1[3])));
      float mn = fminf(fminf(fminf(p0[0], p0[1]), fminf(p0[2], p0[3])), fminf(fminf(p1[0], p1[1]), fminf(p1[2], p1[3])));
      const float l2e = 1.4426950408889634f;
      const float mxs = red4_max(mx) * -l2e, mns = red4_max(-mn) * -l2e;
      const float keep = g == 0 ? 1.0f : 0.0f;
      f32x2 ep[4], en[4];
#pragma unroll
      for (int i = 0; i < 4; ++i) {
        const f32x4 s = i < 2 ? p0 : p1;
        const f32x2 x = f32x2{s[2 * (i & 1)], s[2 * (i & 1) + 1]};
        ep[i] = exp2v(x * l2e + mxs);
        en[i] = exp2v(x * -l2e + mns);
      }
      const f32x2 sp = (ep[0] + ep[1]) + (ep[2] + ep[3]) * keep, sn = (en[0] + en[1]) + (en[2] + en[3]) * keep;
      const float rp = __builtin_amdgcn_rcpf(red4_sum(sp[0] + sp[1])), rn = __builtin_amdgcn_rcpf(red4_sum(sn[0] + sn[1]));
      const float rpk = rp * keep, rnk = rn * keep;
      fp[m] = v4u{cvtpk(ep[0] * rp), cvtpk(ep[1] * rp), cvtpk(ep[2] * rpk), cvtpk(ep[3] * rpk)};
      fn[m] = v4u{cvtpk(en[0] * rn), cvtpk(en[1] * rn), cvtpk(en[2] * rnk), cvtpk(en[3] * rnk)};
    }
    SB0();
    __builtin_amdgcn_s_setprio(OPRIO);
    STAMP_AT(1, 4);
    if (lw == 0) { poll_line(rws, FL_KV + xcd * 128, 0xffffu, tag); lds_arrive(L, C_KVOK); }
    else lds_wait(L, C_KVOK, 1);
    STAMP_AT(1, 5);
    {
      f32x4 v[16];
#pragma unroll
      for (int r = 0; r < 16; ++r)
        v[r] = __builtin_bit_cast(f32x4, __builtin_amdgcn_raw_buffer_load_b128(rws, t * 16, KVP_OFF + (head * 16 + r) * KVP_REC, 16));
      f32x4 s = v[0];
#pragma unroll
      for (int r = 1; r < 16; ++r) s += v[r];
      int d, f0;
      if (t < 128) { d = (t & 63) >> 2; f0 = 16 * (t >> 6) + 4 * (t & 3); }
      else if (t < 160) { d = (t - 128) >> 1; f0 = 32 + 4 * (t & 1); }
      else if (t < 200) { const int u = t < 180 ? t - 160 : t - 180; d = 16 + (u >> 2); f0 = (t < 180 ? 0 : 16) + 4 * (u & 3); }
      else { d = 16 + ((t - 200) >> 1); f0 = 32 + 4 * (t & 1); }
      if (t < 210) *(LAS v2u*)(L + L_KVS + d * 80 + f0 * 2) = cvt4(s);
    }
    lds_arrive(L, C_OSYNC);
    lds_wait(L, C_OSYNC, 4);
    STAMP_AT(1, 6);
    {
      v4u ka[2][2];
#pragma unroll
      for (int b = 0; b < 2; ++b)
#pragma unroll
        for (int st = 0; st < 2; ++st) {
          const int d = b == 0 ? tok : (tok < 4 ? 16 + tok : 20);
          LAS char* src = L + L_KVS + d * 80 + st * 40;
          const v2u lo = *(LAS v2u*)(src + g * 8), hi = *(LAS v2u*)(src + 32);
          ka[b][st] = v4u{lo.x, lo.y, hi.x, hi.y};
        }
      const int rto = rg * 4 + lw;
      const int Q0 = 5 * head + g, Q1 = 5 * head + 4;
      const unsigned o0 = (unsigned)(ACT_OFF + (rto * NKS + NKH + (Q0 >> 3)) * 4096 + (Q0 & 7) * 256 + tok * 16);
      const unsigned o1 = g == 0 ? (unsigned)(ACT_OFF + (rto * NKS + NKH + (Q1 >> 3)) * 4096 + (Q1 & 7) * 256 + tok * 16) : 0x80000000u;
      v2u a[4], b[4];
#pragma unroll
      for (int m = 0; m < 4; ++m) {
        f32x4 n0 = mfma(ka[0][0], fp[m], z4), n1 = mfma(ka[1][0], fp[m], z4);
        n0 = mfma(ka[0][1], fn[m], n0);
        n1 = mfma(ka[1][1], fn[m], n1);
        auto r = __builtin_amdgcn_permlane16_swap(f2u(n1[0]), f2u(n1[0]), false, false);
        const unsigned r1 = r[1];
        const float gm = u2f(__builtin_amdgcn_ds_bpermute((m * 16 + tok) * 4, f2u(gl)));
        const float sc = gm * __builtin_amdgcn_rcpf(u2f(r1) + 1e-6f);
        a[m] = cvt4(n0 * sc); b[m] = cvt4(n1 * sc);
        if (m & 1) {
          st16(rws, o0 + (m >> 1) * 2048, v4u{a[m - 1].x, a[m - 1].y, a[m].x, a[m].y});
          st16(rws, o1 + (m >> 1) * 2048, v4u{b[m - 1].x, b[m - 1].y, b[m].x, b[m].y});
        }
      }
    }
    asm volatile("s_waitcnt vmcnt(0)" ::: "memory");
    if (lane == 0) st8(rws, FL_O + (rg * 4 + lw) * 256 + head * 8, tag);
    STAMP_AT(1, 7);
    if (lw == 1) { poll_line(rws, FL_O + rt64 * 256, 0xffffffffu, tag); lds_arrive(L, C_OOK); }
    if (bid == 0 && lw == 0) {
      for (unsigned spins = 0; spins < (1u << 18); ++spins) {
        bool ok = true;
#pragma unroll
        for (int i = 0; i < 2; ++i) {
          const v4u v = __builtin_amdgcn_raw_buffer_load_b128(rws, vo + i * 1024, FL_ALL, 16);
          ok &= v.x == tag.x && v.y == tag.y && v.z == tag.x && v.w == tag.y;
        }
        if (__all(ok)) break;
        __builtin_amdgcn_s_sleep(POLL_SLEEP);
      }
      if (lane == 0) st8(rws, FL_EPOCH, tag);
    }
    STAMP_FLUSH(1);
    return;
  }

  __builtin_amdgcn_s_setprio(0);
  STAMP_AT(0, 3);

  const __amdgpu_buffer_rsrc_t ra = mk_rsrc(ws + ACT_OFF + rt64 * (NKS * 4096), NKH * 4096);
  const __amdgpu_buffer_rsrc_t rao = mk_rsrc(ws + ACT_OFF + (rt64 * NKS + NKH) * 4096, (NKS - NKH) * 4096);
  const __amdgpu_buffer_rsrc_t rwo = mk_rsrc(ws + WT_OFF + (ct * NKS + NKH) * 5120, (NKS - NKH) * 5120);
  const int h0 = 7 * w, o0 = 3 * (7 - w);
  const unsigned vo = fresh_lane() * 16;
  Stage st[2];
  if (w == 0) {
    __builtin_amdgcn_s_setprio(3);
    poll_line(rws, FL_AUX + xcd * 128, 0xffffu, tag);
    poll_line(rws, FL_RT + (rt64 >> 1) * 128, 0x3ffu, tag);
    lds_arrive(L, C_HOK);
    __builtin_amdgcn_s_setprio(0);
  }
  f32x4 acc[5][4];
  lds_wait(L, C_HOK, 1);
  STAMP_AT(0, 5);
  p2_load_b(st[0], ra, vo, h0);
  p2_load_a(st[0], rw, vo, h0);
  p2_load_b(st[1], ra, vo, h0 + 1);
  p2_load_a(st[1], rw, vo, h0 + 1);
  float gl = 0.f;
  SB0();
#pragma unroll
  for (int i = 0; i < 10; ++i) {
    Stage& cur = st[i & 1];
    if (i < 7) {
#pragma unroll
      for (int nb = 0; nb < 5; ++nb)
#pragma unroll
        for (int m = 0; m < 4; ++m) {
          if (i == 0) mfma_init(acc[nb][m], cur.a[nb], cur.b[m]);
          else mfma_ip2(acc[nb][m], cur.a[nb], cur.b[m]);
        }
      if (i == 6) {
        SB0();
        asm volatile("s_nop 15" ::: "memory");
        SB0();
        const int tk4 = (fresh_lane() & 15) * 4;
#pragma unroll
        for (int m = 0; m < 4; ++m) {
          const float gh = 1.0f - u2f(__builtin_amdgcn_ds_bpermute(m * 64 + tk4, f2u(gl)));
#pragma unroll
          for (int nb = 0; nb < 5; ++nb) acc[nb][m] *= gh;
        }
        SB0();
      }
    } else {
#pragma unroll
      for (int pr = 0; pr < 2; ++pr) {
        const unsigned tx = cur.b[2 * pr].z, ty = cur.b[2 * pr].w;
        cur.b[2 * pr].z = cur.b[2 * pr + 1].x; cur.b[2 * pr].w = cur.b[2 * pr + 1].y;
        cur.b[2 * pr + 1].x = tx; cur.b[2 * pr + 1].y = ty;
      }
#pragma unroll
      for (int nb = 0; nb < 5; ++nb)
#pragma unroll
        for (int m = 0; m < 4; ++m) mfma_ip2(acc[nb][m], cur.a[nb], cur.b[m]);
    }
    SB0();
    const int nx = i + 2;
    if (nx == 6) gl = u2f(__builtin_amdgcn_raw_buffer_load_b32(rws, vo >> 2, G_OFF + rt64 * 256, 16));
    if (nx < 7) { p2_load_b(cur, ra, vo, h0 + nx); p2_load_a(cur, rw, vo, h0 + nx); }
    else if (nx < 10) {
      p2_load_a(cur, rwo, vo, o0 + nx - 7);
      if (nx == 7) lds_wait(L, C_OOK, 1);
      p2_load_b(cur, rao, vo, o0 + nx - 7);
    }
    SB0();
  }
  STAMP_AT(0, 6);
  asm volatile("s_nop 15" ::: "memory");
  lds_wait(L, C_DRAIN, 8);
  SB0();
#pragma unroll
  for (int m = 0; m < 3; ++m)
#pragma unroll
    for (int nb = 0; nb < 5; ++nb) *(LAS f32x4*)(L + ((w * 4 + m) * 5 + nb) * 1024 + vo) = acc[nb][m];
  if (w < 7) {
#pragma unroll
    for (int nb = 0; nb < 5; ++nb) *(LAS f32x4*)(L + ((w * 4 + 3) * 5 + nb) * 1024 + vo) = acc[nb][3];
  }
  SYNCL();
  {
    LANE_VARS(fresh_lane());
    const __amdgpu_buffer_rsrc_t ro_ = mk_rsrc(p.out, S_ * D_ * 2);
    const unsigned op = (unsigned)(((rt64 * 64 + tok) * D_ + ct * 80) * 2);
    if (w < 6) {
      const int m = w % 3, hf = w / 3;
      LAS char* src = L + (m * 5 + hf * 2) * 1024 + vo;
      f32x4 r0 = *(LAS f32x4*)(src), r1 = *(LAS f32x4*)(src + 1024);
#pragma unroll
      for (int sw = 1; sw < 8; ++sw) { r0 += *(LAS f32x4*)(src + sw * 20480); r1 += *(LAS f32x4*)(src + sw * 20480 + 1024); }
      __builtin_amdgcn_raw_buffer_store_b128(cvt8(r0, r1), ro_, op + m * 16 * D_ * 2 + hf * 64 + 16 * g, 0, 16);
    } else if (w == 6) {
#pragma unroll
      for (int m = 0; m < 3; ++m) {
        LAS char* src = L + (m * 5 + 4) * 1024 + vo;
        f32x4 r = *(LAS f32x4*)(src);
#pragma unroll
        for (int sw = 1; sw < 8; ++sw) r += *(LAS f32x4*)(src + sw * 20480);
        __builtin_amdgcn_raw_buffer_store_b64(cvt4(r), ro_, op + m * 16 * D_ * 2 + 128 + 8 * g, 0, 16);
      }
    } else {
#pragma unroll
      for (int sw = 0; sw < 7; ++sw)
#pragma unroll
        for (int nb = 0; nb < 5; ++nb) acc[nb][3] += *(LAS f32x4*)(L + ((sw * 4 + 3) * 5 + nb) * 1024 + vo);
      __builtin_amdgcn_raw_buffer_store_b128(cvt8(acc[0][3], acc[1][3]), ro_, op + 3 * 16 * D_ * 2 + 16 * g, 0, 16);
      __builtin_amdgcn_raw_buffer_store_b128(cvt8(acc[2][3], acc[3][3]), ro_, op + 3 * 16 * D_ * 2 + 64 + 16 * g, 0, 16);
      __builtin_amdgcn_raw_buffer_store_b64(cvt4(acc[4][3]), ro_, op + 3 * 16 * D_ * 2 + 128 + 8 * g, 0, 16);
    }
  }
  STAMP_AT(0, 7);
  STAMP_FLUSH(0);
}

extern "C" void kernel_launch(void* const* d_in, const int* in_sizes, int n_in, void* d_out, int out_size, void* d_ws,
                              size_t ws_size, hipStream_t stream) {
  Args a;
  a.x = (const u16*)d_in[0]; a.wqkv = (const u16*)d_in[1]; a.wphi = (const u16*)d_in[2]; a.wo = (const u16*)d_in[3];
  a.wg = (const u16*)d_in[5]; a.wu = (const u16*)d_in[6]; a.wd = (const u16*)d_in[7]; a.gw = (const u16*)d_in[8];
  a.ws = (char*)d_ws;
  a.out = (u16*)d_out;
  (void)hipFuncSetAttribute((const void*)fused, hipFuncAttributeMaxDynamicSharedMemorySize, K1_LDS);
  fused<<<256, 768, K1_LDS, stream>>>(a);
}
```

```cpp
#include <hip/hip_runtime.h>

typedef unsigned short u16;
typedef unsigned v4u __attribute__((ext_vector_type(4)));
typedef unsigned v2u __attribute__((ext_vector_type(2)));
typedef float f32x4 __attribute__((ext_vector_type(4)));
typedef float f32x2 __attribute__((ext_vector_type(2)));
typedef short s16x4 __attribute__((ext_vector_type(4)));
typedef __bf16 bf16x8 __attribute__((ext_vector_type(8)));
typedef __bf16 bf16x2 __attribute__((ext_vector_type(2)));
#define LAS __attribute__((address_space(3)))
#define GAS __attribute__((address_space(1)))
#define DI __device__ __forceinline__

constexpr int S_ = 2048, D_ = 640, F_ = 1728;
constexpr int NKS = 74, NKH = 54;
constexpr unsigned ACT_OFF = 0, ACT_BYTES = 32u * NKS * 4 * 1024;
constexpr unsigned WT_OFF = ACT_OFF + ACT_BYTES, WT_BYTES = 8u * NKS * 5 * 1024;
constexpr unsigned Q_OFF = WT_OFF + WT_BYTES, Q_BYTES = 32u * 128 * 1024;
constexpr unsigned KVP_REC = 3360;
constexpr unsigned KVP_OFF = Q_OFF + Q_BYTES, KVP_BYTES = 32u * 16 * KVP_REC;
constexpr unsigned G_OFF = KVP_OFF + KVP_BYTES, G_BYTES = S_ * 4;
constexpr unsigned WPF_OFF = G_OFF + G_BYTES, WPF_BYTES = 32u * 2 * 1024;
constexpr unsigned FL_EPOCH = WPF_OFF + WPF_BYTES, FL_ALL = FL_EPOCH + 256, FL_RT = FL_ALL + 2048, FL_AUX = FL_RT + 16 * 128,
                   FL_KV = FL_AUX + 8 * 128, FL_Q = FL_KV + 8 * 128, FL_O = FL_Q + 8 * 256;
constexpr unsigned WS_BYTES = FL_O + 32 * 256;
constexpr unsigned SENT = 0xFFFFFFFFu;

constexpr unsigned L_W = 0, WSLOT = 24576, L_X = 4 * WSLOT, XSLOT = 16384, L_WPHI = L_X + 2 * XSLOT, L_ZERO = L_WPHI + 6400,
                   L_DUMMY = L_WPHI + 8192, K1_LDS = 163840;
constexpr unsigned L_KVS = 161792, L_CTL = 163584;
enum { C_TAG = 0, C_LOOP = 8, C_DRAIN = 12, C_X = 16, C_KV1 = 32, C_KV2 = 36, C_AUXOK = 40, C_PRE = 44, C_CONS = 48, C_OSYNC = 52, C_KVOK = 56, C_T1 = 60, C_HOK = 64, C_T2 = 68, C_OOK = 72 };
constexpr unsigned L_PK = 0, L_V = 8 * 128 * 80;

DI int fresh_lane() {
  int l;
  asm volatile("v_mbcnt_lo_u32_b32 %0, -1, 0\n\tv_mbcnt_hi_u32_b32 %0, -1, %0" : "=v"(l));
  return l;
}
#define LANE_VARS(l) const int lane = (l), g = lane >> 4, q = (lane >> 2) & 3, pp = lane & 3, tok = lane & 15; (void)q; (void)pp; (void)tok; (void)g
DI unsigned rfl(unsigned x) { return __builtin_amdgcn_readfirstlane(x); }
DI unsigned cvtpk(f32x2 v) {
  bf16x2 b = __builtin_convertvector(v, bf16x2);
  return __builtin_bit_cast(unsigned, b);
}
DI unsigned cvtpk(float lo, float hi) { return cvtpk(f32x2{lo, hi}); }
DI v2u cvt4(f32x4 v) { return v2u{cvtpk(v[0], v[1]), cvtpk(v[2], v[3])}; }
DI v4u cvt8(f32x4 a, f32x4 b) { return v4u{cvtpk(a[0], a[1]), cvtpk(a[2], a[3]), cvtpk(b[0], b[1]), cvtpk(b[2], b[3])}; }
DI float bf2f(unsigned b) { return __builtin_bit_cast(float, b << 16); }
DI f32x4 mfma(v4u a, v4u b, f32x4 c) {
  return __builtin_amdgcn_mfma_f32_16x16x32_bf16(__builtin_bit_cast(bf16x8, a), __builtin_bit_cast(bf16x8, b), c, 0, 0, 0);
}
DI v2u tr_read(LAS char* p) {
  s16x4 r = __builtin_amdgcn_ds_read_tr16_b64_v4i16((LAS s16x4*)p);
  return __builtin_bit_cast(v2u, r);
}
DI v4u tr_read2(LAS char* p, int d) {
  v2u a = tr_read(p), b = tr_read(p + d);
  return v4u{a.x, a.y, b.x, b.y};
}
DI __amdgpu_buffer_rsrc_t mk_rsrc(const void* p, unsigned bytes) {
  return __builtin_amdgcn_make_buffer_rsrc((void*)p, (short)0, (int)bytes, 0x00020000);
}
DI float u2f(unsigned u) { return __builtin_bit_cast(float, u); }
DI unsigned f2u(float f) { return __builtin_bit_cast(unsigned, f); }
DI float red4_max(float x) {
  auto r = __builtin_amdgcn_permlane16_swap(f2u(x), f2u(x), false, false);
  unsigned r0 = r[0], r1 = r[1];
  x = fmaxf(u2f(r0), u2f(r1));
  auto t = __builtin_amdgcn_permlane32_swap(f2u(x), f2u(x), false, false);
  r0 = t[0]; r1 = t[1];
  return fmaxf(u2f(r0), u2f(r1));
}
DI float red4_sum(float x) {
  auto r = __builtin_amdgcn_permlane16_swap(f2u(x), f2u(x), false, false);
  unsigned r0 = r[0], r1 = r[1];
  x = u2f(r0) + u2f(r1);
  auto t = __builtin_amdgcn_permlane32_swap(f2u(x), f2u(x), false, false);
  r0 = t[0]; r1 = t[1];
  return u2f(r0) + u2f(r1);
}
DI f32x2 exp2v(f32x2 a) { return f32x2{__builtin_amdgcn_exp2f(a[0]), __builtin_amdgcn_exp2f(a[1])}; }
DI void st16(__amdgpu_buffer_rsrc_t r, unsigned off, v4u v) { __builtin_amdgcn_raw_buffer_store_b128(v, r, off, 0, 16); }
DI void st8(__amdgpu_buffer_rsrc_t r, unsigned off, v2u v) { __builtin_amdgcn_raw_buffer_store_b64(v, r, off, 0, 16); }
#define AUX_DELAY 3
#define PF_MODE 1
#define PF_D0 8
#define PF_D1 1
#define SB0() __builtin_amdgcn_sched_barrier(0)
#define SYNC(n) do { SB0(); asm volatile("s_waitcnt vmcnt(" #n ") lgkmcnt(0)\n\ts_barrier" ::: "memory"); SB0(); } while (0)
#define NT_A "nt"
#define LDSDMA(m0v, vo, rs, so) \
  asm volatile("s_mov_b32 m0, %0\n\ts_nop 0\n\tbuffer_load_dwordx4 %1, %2, %3 offen " NT_A " lds" :: "s"(m0v), "v"(vo), "s"(rs), "s"(so) : "memory")

#define STAMP_DECL
#define STAMP_AT(kern, i)
#define STAMP_FLUSH(kern)
#define STAMP_ARG
#define STAMP_PASS

struct Args {
  const u16 *x, *wqkv, *wphi, *wo, *wg, *wu, *wd, *gw;
  char* ws;
  u16* out;
};

#define SYNCL() do { SB0(); asm volatile("s_waitcnt lgkmcnt(0)\n\ts_barrier" ::: "memory"); SB0(); } while (0)
#define NT_D ""
#define NT_P ""
#define DMA(m0v, vo, rs, so, off) \
  asm volatile("s_mov_b32 m0, %0\n\ts_nop 0\n\tbuffer_load_dwordx4 %1, %2, %3 offen offset:%4 " NT_D " lds" :: "s"(m0v), "v"(vo), "s"(rs), "s"(so), "i"(off) : "memory")
#define DMAS(m0v, vo, rs, so, off) \
  asm volatile("s_mov_b32 m0, %0\n\ts_nop 0\n\tbuffer_load_dwordx4 %1, %2, %3 offen offset:%4 sc1 lds" :: "s"(m0v), "v"(vo), "s"(rs), "s"(so), "i"(off) : "memory")

template <int MODE> struct MI { static constexpr int NB = MODE == 1 ? 5 : 6; };
template <int MODE, int B, int R, int SLOT> DI v2u a_read(LAS char* L, unsigned (&aoff)[6]) {
  constexpr int I = MODE == 0 ? B >> 1 : B;
  if constexpr (MODE == 2 && B == 5) {
    const v2u t = *(LAS v2u*)(L + aoff[5] + SLOT * 64 + R * 8);
    if constexpr (R == 1 && SLOT == 3) asm volatile("v_add_u32 %0, 0x100, %0" : "+v"(aoff[5]));
    return t;
  }
  const v2u t = tr_read(L + L_W + (SLOT & 1) * WSLOT + aoff[I] + ((MODE == 0 && (B & 1)) ? 12288 : 0) + R * 1536);
  if constexpr (R == 1 && (MODE != 0 || (B & 1))) {
    if constexpr (SLOT == 1) asm volatile("v_add_u32 %0, 0xc000, %0" : "+v"(aoff[I]));
    if constexpr (SLOT == 3) asm volatile("v_subrev_u32 %0, 0xc000, %0" : "+v"(aoff[I]));
  }
  return t;
}
DI void mfma_ip(f32x4& c, const v4u& a, const v4u& b) {
  asm volatile("v_mfma_f32_16x16x32_bf16 %0, %1, %2, %0" : "+v"(c) : "v"(a), "v"(b));
}
template <int MODE, int T, int J = 0>
DI void k1_step(f32x4 (&acc)[4][MI<MODE>::NB], v4u (&a)[4], v4u (&x)[2][4], LAS char* L, unsigned (&aoff)[6],
                const unsigned (&xoff)[2]) {
  constexpr int NB = MI<MODE>::NB, R0 = T * NB;
  if constexpr (J < 4 * NB) {
    constexpr int b = J >> 2, r = J & 3, nb = b + 2;
    if constexpr (r < 2) {
      v2u t = a_read<MODE, nb % NB, r, (nb < NB ? T : T + 1) & 3>(L, aoff);
      if constexpr (r == 0) { a[(R0 + nb) & 3].x = t.x; a[(R0 + nb) & 3].y = t.y; }
      else { a[(R0 + nb) & 3].z = t.x; a[(R0 + nb) & 3].w = t.y; }
    } else if constexpr (r == 2 && b < 4)
      x[(T + 1) & 1][b] = *(LAS v4u*)(L + xoff[(T + 1) & 1] + ((((T + 1) >> 1) & 1) * XSLOT + b * 2048));
    mfma_ip(acc[r][b], a[(R0 + b) & 3], x[T & 1][r]);
    SB0();
    k1_step<MODE, T, J + 1>(acc, a, x, L, aoff, xoff);
  }
}
template <int MODE>
DI void k1_mainloop(f32x4 (&acc)[4][MI<MODE>::NB], LAS char* L, unsigned (&aoff)[6], unsigned xoff0 STAMP_ARG) {
  unsigned xoff[2] = {xoff0 + L_X, (xoff0 ^ 64) + L_X};
  asm volatile("" : "+v"(xoff[0]), "+v"(xoff[1]));
  v4u a[4], x[2][4];
#pragma unroll
  for (int m = 0; m < 4; ++m)
#pragma unroll
    for (int b = 0; b < MI<MODE>::NB; ++b) acc[m][b] = f32x4{0.f, 0.f, 0.f, 0.f};
  SYNCL();
  STAMP_AT(0, 4);
#pragma unroll
  for (int m = 0; m < 4; ++m) x[0][m] = *(LAS v4u*)(L + xoff[0] + m * 2048);
  {
    const v2u t0 = a_read<MODE, 0, 0, 0>(L, aoff), t1 = a_read<MODE, 0, 1, 0>(L, aoff);
    const v2u t2 = a_read<MODE, 1, 0, 0>(L, aoff), t3 = a_read<MODE, 1, 1, 0>(L, aoff);
    a[0] = v4u{t0.x, t0.y, t1.x, t1.y}; a[1] = v4u{t2.x, t2.y, t3.x, t3.y};
  }
#pragma unroll 1
  for (int i = 0; i < 20; i += 4) {
    SYNCL(); k1_step<MODE, 0>(acc, a, x, L, aoff, xoff);
    SYNCL(); k1_step<MODE, 1>(acc, a, x, L, aoff, xoff);
    SYNCL(); k1_step<MODE, 2>(acc, a, x, L, aoff, xoff);
    SYNCL(); k1_step<MODE, 3>(acc, a, x, L, aoff, xoff);
  }
  asm volatile("s_nop 15" ::: "memory");
  __builtin_amdgcn_s_setprio(2);
  SB0();
}

DI unsigned gelu_mul2(f32x2 x, f32x2 u) {
  const float k0 = -2.0f * 0.7978845608028654f * 1.4426950408889634f, k1 = k0 * 0.044715f;
  f32x2 t = x * x;
  f32x2 a = x * (t * k1 + k0);
  f32x2 d = exp2v(a) + 1.0f;
  f32x2 r = f32x2{__builtin_amdgcn_rcpf(d[0]), __builtin_amdgcn_rcpf(d[1])};
  return cvtpk((x * u) * r);
}
DI v2u gelu_mul4(f32x4 x, f32x4 u) {
  return v2u{gelu_mul2(f32x2{x[0], x[1]}, f32x2{u[0], u[1]}), gelu_mul2(f32x2{x[2], x[3]}, f32x2{u[2], u[3]})};
}

#define OPRIO 3
#define CODE_PF 49152
#define PFV 2
#define POLL_SLEEP 6
DI void lds_arrive(LAS char* L, unsigned word) {
  asm volatile("s_waitcnt lgkmcnt(0)" ::: "memory");
  if (fresh_lane() == 0) __hip_atomic_fetch_add((LAS unsigned*)(L + L_CTL + word), 1u, __ATOMIC_RELAXED, __HIP_MEMORY_SCOPE_WORKGROUP);
}
DI void lds_wait(LAS char* L, unsigned word, unsigned target) {
  volatile LAS unsigned* f = (volatile LAS unsigned*)(L + L_CTL + word);
  for (unsigned spins = 0; rfl(*f) < target && spins < (1u << 22); ++spins) __builtin_amdgcn_s_sleep(1);
  asm volatile("" ::: "memory");
}
DI void poll_line(__amdgpu_buffer_rsrc_t rws, unsigned line, unsigned mask, v2u tag) {
  const int l = fresh_lane() & 31;
  const bool need = (mask >> l) & 1u;
  for (unsigned spins = 0; spins < (1u << 18); ++spins) {
    const v2u v = __builtin_amdgcn_raw_buffer_load_b64(rws, l * 8, line, 16);
    if (!__any(need && (v.x != tag.x || v.y != tag.y))) break;
    __builtin_amdgcn_s_sleep(POLL_SLEEP);
  }
  __builtin_amdgcn_fence(__ATOMIC_ACQUIRE, "wavefront");
}

DI void poll_once(LAS char* L, unsigned ticket, unsigned okw, __amdgpu_buffer_rsrc_t rws, unsigned line, unsigned mask, v2u tag) {
  unsigned t = 0;
  if (fresh_lane() == 0) t = __hip_atomic_fetch_add((LAS unsigned*)(L + L_CTL + ticket), 1u, __ATOMIC_RELAXED, __HIP_MEMORY_SCOPE_WORKGROUP);
  if (rfl(t) == 0) { poll_line(rws, line, mask, tag); lds_arrive(L, okw); }
  else lds_wait(L, okw, 1);
}
DI void mfma_ip2(f32x4& c, const v4u& a, const v4u& b) {
  asm volatile("s_nop 1\n\tv_mfma_f32_16x16x32_bf16 %0, %1, %2, %0" : "+v"(c) : "v"(a), "v"(b));
}
DI void mfma_init(f32x4& c, const v4u& a, const v4u& b) {
  asm volatile("s_nop 1\n\tv_mfma_f32_16x16x32_bf16 %0, %1, %2, 0" : "=&v"(c) : "v"(a), "v"(b));
}
struct Stage { v4u a[5], b[4]; };
DI void p2_load_b(Stage& st, __amdgpu_buffer_rsrc_t ra, unsigned vo, int ks) {
#pragma unroll
  for (int i = 0; i < 4; ++i) st.b[i] = __builtin_amdgcn_raw_buffer_load_b128(ra, vo + i * 1024, ks * 4096, 16);
}
DI void p2_load_a(Stage& st, __amdgpu_buffer_rsrc_t rw, unsigned vo, int ks) {
#pragma unroll
  for (int nb = 0; nb < 5; ++nb) st.a[nb] = __builtin_amdgcn_raw_buffer_load_b128(rw, vo + nb * 1024, ks * 5120, 16);
}

__global__ __launch_bounds__(768, 3) void fused(Args p) {
  extern __shared__ __attribute__((aligned(16))) char smem[];
  LAS char* L = (LAS char*)smem;
  const int w = rfl(threadIdx.x >> 6);
  const int bid = blockIdx.x, xcd = bid & 7, idx = bid >> 3;
  const unsigned long long CSMAP = 0xDC87BA65E943F210ull;
  const int rt = (xcd & 1) * 8 + (idx & 7), cs = (int)(CSMAP >> (4 * ((xcd >> 1) * 4 + (idx >> 3)))) & 15;
  char* ws = p.ws;
  const __amdgpu_buffer_rsrc_t rws = mk_rsrc(ws, WS_BYTES);
  STAMP_DECL;
  STAMP_AT(0, 0);
  const bool isAUX = cs == 15, isGU = cs < 9, isKV = cs >= 11 && cs < 15;

  if (w == 1) {
    unsigned long long pc;
    asm volatile("s_getpc_b64 %0" : "=s"(pc));
    const __amdgpu_buffer_rsrc_t rc = mk_rsrc((const void*)(pc & ~255ull), CODE_PF);
    DMA(L_DUMMY + 4096, (unsigned)(idx * 2048 + fresh_lane() * 16), rc, 0, 0);
    DMA(L_DUMMY + 4096, (unsigned)(idx * 2048 + fresh_lane() * 16), rc, 0, 1024);
  }
  if (w == 0) {
    const int lane = fresh_lane();
    const v2u e = __builtin_amdgcn_raw_buffer_load_b64(rws, 0, FL_EPOCH, 16);
    unsigned long long t = (((unsigned long long)e.y << 32) | e.x) * 0x9E3779B97F4A7C15ull + 0xD1B54A32D192ED03ull;
    t ^= t >> 29;
    if (lane < 32) *(LAS unsigned*)(L + L_CTL + lane * 4) = lane == 0 ? (unsigned)t : lane == 1 ? (unsigned)(t >> 32) : 0u;
  }

  if (isAUX) {
    LANE_VARS(fresh_lane());
    if (w >= 8) {
      SYNCL();
    } else {
      const __amdgpu_buffer_rsrc_t rd = mk_rsrc(p.wd, F_ * D_ * 2), ro = mk_rsrc(p.wo, D_ * D_ * 2);
      unsigned vlo[5];
#pragma unroll
      for (int j = 0; j < 5; ++j) {
        const int Lc = ((w & 3) * 5 + j) * 64 + lane;
        vlo[j] = (unsigned)((Lc / 80) * 1280 + (Lc % 80) * 16);
      }
      auto issue = [&](int sl) {
        const int s = rt + 16 * sl;
        int b0, b1;
        if (s < NKH) {
          const int u = s / 6, e = s % 6;
          if (e < 4) { b0 = 192 * u + 48 * e; b1 = b0 + 16; } else { b0 = 192 * u + 96 * (e - 4) + 32; b1 = b0 + 48; }
        } else { b0 = 32 * (s - NKH); b1 = b0 + 16; }
        const __amdgpu_buffer_rsrc_t rs = s < NKH ? rd : ro;
        const unsigned so = (unsigned)((w < 4 ? b0 : b1) * 1280), m0 = (unsigned)((sl % 3) * 40960 + w * 5120);
#pragma unroll
        for (int j = 0; j < 5; ++j) LDSDMA(m0 + j * 1024, vlo[j], rs, so);
      };
      auto retile = [&](int sl) {
        const int s = rt + 16 * sl;
        LAS char* base = L + (sl % 3) * 40960 + (4 * g + q) * 1280 + w * 160;
        const unsigned dst = s < NKS ? WT_OFF + (w * NKS + s) * 5120 + lane * 16 : 0x80000000u;
#pragma unroll
        for (int nb = 0; nb < 5; ++nb) {
          const int col = nb < 4 ? 32 * (nb >> 1) + 8 * pp + 4 * (nb & 1) : 64 + 4 * pp;
          st16(rws, dst + nb * 1024, tr_read2(base + col * 2, 16 * 1280));
        }
      };
      SYNCL();
      for (int i = 0; i < AUX_DELAY; ++i) __builtin_amdgcn_s_sleep(16);
#define AUX_SYNC(n, k) do { asm volatile("s_waitcnt vmcnt(" #n ")" ::: "memory"); lds_arrive(L, C_KV1); lds_wait(L, C_KV1, 8 * (k)); } while (0)
      issue(0); issue(1); issue(2);
      AUX_SYNC(10, 1); retile(0);
      AUX_SYNC(10, 2); issue(3); retile(1);
      AUX_SYNC(15, 3); issue(4); retile(2);
      AUX_SYNC(15, 4); retile(3);
      AUX_SYNC(10, 5); retile(4);
    }
  } else {
    const __amdgpu_buffer_rsrc_t rx = mk_rsrc(p.x + rt * 128 * D_, (unsigned)((S_ - rt * 128) * D_ * 2));
    const unsigned pitch = isGU ? F_ * 2 : 3 * D_ * 2, total = D_ * pitch;
    unsigned boffA, boffB;
    if (isGU) boffA = boffB = 384u * cs;
    else if (!isKV) { boffA = 640u * (cs - 9); boffB = boffA + 256u; }
    else { const int j = cs - 11; boffA = 1280u + 320u * j - ((j & 1) ? 64u : 0u); boffB = boffA + 1280u; }
    const char* baseA = (const char*)(isGU ? p.wg : p.wqkv) + boffA;
    const char* baseB = (const char*)(isGU ? p.wu : p.wqkv) + boffB;
    const int mh = w & 1, n = w >> 1;
    const int rt64 = rt * 2 + mh;
    if (w >= 8) {
      __builtin_amdgcn_s_setprio(3);
      const int lane = fresh_lane();
      const int lw = w - 8, sub = lw >> 1;
      const int r0 = 32 * lw + (lane >> 3);
      const unsigned vx0 = (unsigned)(r0 * 1280 + (((lane & 7) ^ ((r0 >> 1) & 7)) * 16)), vx1 = (vx0 ^ 64u) + 8 * 1280 - 1024;
      const unsigned m0x = L_X + lw * 4096;
#define LOAD_X(stage, slot, e) do { const unsigned m0_ = m0x + (unsigned)(slot) * XSLOT + (e) * 2048, so_ = (unsigned)(stage) * 128 + (e) * 16 * 1280; \
      DMA(m0_, vx0, rx, so_, 0); DMA(m0_, vx1, rx, so_, 1024); } while (0)
#define LOAD_XX(stage) do { LOAD_X(stage, (stage) & 1, 0); LOAD_X(stage, (stage) & 1, 1); } while (0)
      if (cs == 9 && lw < 2) {
        const __amdgpu_buffer_rsrc_t rg = mk_rsrc(p.gw, D_ * 2);
        DMA(L_WPHI + lw * 1024, (unsigned)(lw * 1024 + lane * 16), rg, 0, 0);
      }
      LOAD_XX(0);
      const __amdgpu_buffer_rsrc_t rw = mk_rsrc((sub ? baseB : baseA) + (lw & 1) * 16 * pitch, total - (sub ? boffB : boffA) - (lw & 1) * 16 * pitch);
      unsigned vw[6];
#pragma unroll
      for (int j = 0; j < 3; ++j) {
        const int Lc = 64 * j + lane, kr = Lc / 24, pos = Lc % 24;
        const unsigned c0 = (unsigned)((pos >> 3) * 128 + (((pos & 7) ^ (kr & 2)) * 16));
        vw[j] = (unsigned)kr * pitch + c0 - 1024u * j;
        vw[3 + j] = (unsigned)(kr + 8) * pitch + (c0 ^ 64u) - 1024u * j;
      }
      const unsigned m0w = L_W + sub * 12288 + (lw & 1) * 6144, wstep = 32 * pitch;
#define LOAD_W(s) do { const unsigned m0_ = m0w + (unsigned)((s) & 3) * WSLOT, so_ = (unsigned)(s) * wstep; \
      DMA(m0_, vw[0], rw, so_, 0); DMA(m0_, vw[1], rw, so_, 1024); DMA(m0_, vw[2], rw, so_, 2048); \
      DMA(m0_ + 3072, vw[3], rw, so_, 0); DMA(m0_ + 3072, vw[4], rw, so_, 1024); DMA(m0_ + 3072, vw[5], rw, so_, 2048); } while (0)
      const __amdgpu_buffer_rsrc_t rpf = lw == 3 ? rx : mk_rsrc(((rt & 1) ? baseB : baseA) + 8 * (rt >> 1) * pitch, total - ((rt & 1) ? boffB : boffA) - 8 * (rt >> 1) * pitch);
      const unsigned vpf = lw == 3 ? (unsigned)((8 * cs + (lane >> 3)) * 1280 + (lane & 7) * 16) : lw == 0 ? vw[0] : lw == 1 ? vw[1] + 1024u : vw[2] + 2048u;
      const unsigned pfstep = lw == 3 ? 128u : 64u * pitch;
#define PF(stage) DMA(L_DUMMY + lw * 1024, vpf, rpf, (unsigned)(stage) * pfstep, 0)
      LOAD_W(0);
      LOAD_W(1); LOAD_XX(1);
      LOAD_W(2); PF(2); PF(3);
      SYNC(18);
      SYNC(8); LOAD_W(3); PF(4);
      SYNC(9); LOAD_XX(2); LOAD_W(4);
      SYNC(11); LOAD_W(5); PF(5);
      SYNC(7); LOAD_XX(3); LOAD_W(6);
      SYNC(11); LOAD_W(7); PF(6); PF(7);
      SYNC(8); LOAD_XX(4); LOAD_W(8);
      SYNC(12); LOAD_W(9); PF(8); PF(9);
      SYNC(8); LOAD_XX(5); LOAD_W(10);
      asm volatile("s_waitcnt vmcnt(12)" ::: "memory");
      SYNC(10); LOAD_W(11);
#pragma unroll 1
      for (int i = 9; i < 15; i += 2) {
        SYNC(6); LOAD_X((i + 3) >> 1, ((i + 3) >> 1) & 1, 0); LOAD_X((i + 3) >> 1, ((i + 3) >> 1) & 1, 1); LOAD_W(i + 3);
        SYNC(10); LOAD_W(i + 4);
      }
      SYNC(6); LOAD_XX(9); LOAD_W(18);
      SYNC(10); LOAD_W(19);
      if (isKV) {
        const __amdgpu_buffer_rsrc_t rp = mk_rsrc(p.wphi + 8 * (cs - 11) * 400, 6400);
        DMA(L_WPHI + lw * 2048, (unsigned)(lw * 2048 + lane * 16), rp, 0, 0);
        DMA(L_WPHI + lw * 2048, (unsigned)(lw * 2048 + lane * 16), rp, 0, 1024);
      }
      SYNC(6);
      SYNC(0);
      SYNC(0);
    } else if (isGU) {
#define FRAG_VARS LANE_VARS(fresh_lane()); const int sw = ((q >> 1) & 1) | ((g & 1) << 1); \
  const unsigned xoff0 = (unsigned)((64 * mh + tok) * 128 + ((g ^ (tok >> 1)) * 16))
      f32x4 acc[4][6];
      {
        FRAG_VARS;
        unsigned aoff[6];
#pragma unroll
        for (int b = 0; b < 3; ++b) {
          const int ch = 6 * n + 2 * b + (pp >> 1);
          aoff[b] = (unsigned)((8 * g + q) * 384 + (ch >> 3) * 128 + (((ch & 7) ^ (2 * sw)) * 16) + (pp & 1) * 8);
        }
        aoff[3] = aoff[4] = aoff[5] = 0;
        k1_mainloop<0>(acc, L, aoff, xoff0 STAMP_PASS);
      }
      STAMP_AT(0, 1);
      const int lane = fresh_lane();
      const unsigned actA = ACT_OFF + ((rt64 * NKS + 6 * cs + n) * 4) * 1024 + lane * 16;
      const unsigned actB = ACT_OFF + ((rt64 * NKS + 6 * cs + 4 + (n >> 1)) * 4 + 2 * (n & 1)) * 1024 + lane * 16;
      v2u h2[4];
#pragma unroll
      for (int m = 0; m < 4; ++m) h2[m] = gelu_mul4(acc[m][4], acc[m][5]);
      const bool odd = n & 1;
      *(LAS v4u*)(L + L_WPHI + w * 1024 + lane * 16) = odd ? v4u{h2[0].x, h2[0].y, h2[1].x, h2[1].y} : v4u{h2[2].x, h2[2].y, h2[3].x, h2[3].y};
      const unsigned xw = C_X + 4 * ((w & 1) | ((w >> 2) << 1));
      lds_arrive(L, xw);
      const v2u k0 = odd ? h2[2] : h2[0], k1 = odd ? h2[3] : h2[1];
#pragma unroll
      for (int m = 0; m < 4; ++m) {
        const v2u h0 = gelu_mul4(acc[m][0], acc[m][1]), h1 = gelu_mul4(acc[m][2], acc[m][3]);
        st16(rws, actA + m * 1024, v4u{h0.x, h0.y, h1.x, h1.y});
        if (m == 2) {
          lds_wait(L, xw, 2);
          const v4u o = *(LAS v4u*)(L + L_WPHI + (w ^ 2) * 1024 + lane * 16);
          st16(rws, actB, odd ? v4u{o.x, o.y, k0.x, k0.y} : v4u{k0.x, k0.y, o.x, o.y});
          st16(rws, actB + 1024, odd ? v4u{o.z, o.w, k1.x, k1.y} : v4u{k1.x, k1.y, o.z, o.w});
        }
      }
    } else {
#define QKV_AOFF \
    FRAG_VARS; \
    unsigned aoff[6]; \
    { \
      aoff[5] = L_WPHI + g * 16; \
      const int offch = isKV ? ((cs - 11) & 1) * 4 : (n >> 1) * 4; \
      _Pragma("unroll") for (int b = 0; b < 5; ++b) { \
        const int u = 2 * offch + 20 * (n & 1) + 5 * pp + b, ch = u >> 1; \
        aoff[b] = (unsigned)((n >> 1) * 12288 + (8 * g + q) * 384 + (ch >> 3) * 128 + (((ch & 7) ^ (2 * sw)) * 16) + (u & 1) * 8); \
      } \
    }
      v4u c01[4], c23[4];
      v2u c4[4];
      if (cs == 9) {
        f32x4 acc[4][6];
        {
          QKV_AOFF
          k1_mainloop<2>(acc, L, aoff, xoff0 STAMP_PASS);
        }
#pragma unroll
        for (int m = 0; m < 4; ++m) { c01[m] = cvt8(acc[m][0], acc[m][1]); c23[m] = cvt8(acc[m][2], acc[m][3]); c4[m] = cvt4(acc[m][4]); }
        if (n == 0) {
          LANE_VARS(fresh_lane());
          const float a = g == 0 ? acc[0][5][0] : g == 1 ? acc[1][5][0] : g == 2 ? acc[2][5][0] : acc[3][5][0];
          __builtin_amdgcn_raw_buffer_store_b32(f2u(1.0f / (1.0f + __expf(-a))), rws, G_OFF + (rt64 * 64 + lane) * 4, 0, 16);
        }
      } else {
        f32x4 acc[4][5];
        {
          QKV_AOFF
          k1_mainloop<1>(acc, L, aoff, xoff0 STAMP_PASS);
        }
#pragma unroll
        for (int m = 0; m < 4; ++m) { c01[m] = cvt8(acc[m][0], acc[m][1]); c23[m] = cvt8(acc[m][2], acc[m][3]); c4[m] = cvt4(acc[m][4]); }
      }
      STAMP_AT(0, 1);
      LANE_VARS(fresh_lane());
      if (!isKV) {
          const int head = 16 * (cs - 9) + 4 * n + g;
        const unsigned qp = Q_OFF + (head * 32 + rt64) * 2560;
#pragma unroll
        for (int pr = 0; pr < 2; ++pr) {
          const unsigned o = qp + pr * 1024 + tok * 16;
          st16(rws, o, v4u{c01[2 * pr].x, c01[2 * pr].y, c01[2 * pr + 1].x, c01[2 * pr + 1].y});
          st16(rws, o + 256, v4u{c01[2 * pr].z, c01[2 * pr].w, c01[2 * pr + 1].z, c01[2 * pr + 1].w});
          st16(rws, o + 512, v4u{c23[2 * pr].x, c23[2 * pr].y, c23[2 * pr + 1].x, c23[2 * pr + 1].y});
          st16(rws, o + 768, v4u{c23[2 * pr].z, c23[2 * pr].w, c23[2 * pr + 1].z, c23[2 * pr + 1].w});
        }
        const unsigned o = qp + 2048 + tok * 8;
        st8(rws, o, v2u{(c4[0].x & 0xffffu) | (c4[1].x << 16), (c4[2].x & 0xffffu) | (c4[3].x << 16)});
        st8(rws, o + 128, v2u{(c4[0].x >> 16) | (c4[1].x & 0xffff0000u), (c4[2].x >> 16) | (c4[3].x & 0xffff0000u)});
        st8(rws, o + 256, v2u{(c4[0].y & 0xffffu) | (c4[1].y << 16), (c4[2].y & 0xffffu) | (c4[3].y << 16)});
        st8(rws, o + 384, v2u{(c4[0].y >> 16) | (c4[1].y & 0xffff0000u), (c4[2].y >> 16) | (c4[3].y & 0xffff0000u)});
      } else {
        const int hl = 4 * (n & 1) + g;
        LAS char* xch = L + L_DUMMY + (w & 3) * 5120;
        if (n < 2) {
#pragma unroll
          for (int mm = 0; mm < 2; ++mm) {
            *(LAS v4u*)(xch + mm * 2560 + lane * 16) = c01[2 + mm];
            *(LAS v4u*)(xch + mm * 2560 + 1024 + lane * 16) = c23[2 + mm];
            *(LAS v2u*)(xch + mm * 2560 + 2048 + lane * 8) = c4[2 + mm];
          }
        }
        lds_arrive(L, C_KV1);
        v4u wf[5][3];
        {
          LAS char* wb = L + (pp == g ? L_WPHI + (unsigned)(hl * 800 + q * 40) : L_ZERO);
#pragma unroll
          for (int bo = 0; bo < 5; ++bo)
#pragma unroll
            for (int s3 = 0; s3 < 3; ++s3) {
              v2u lo = tr_read(wb + s3 * 320 + bo * 8), hi = v2u{0u, 0u};
              if (s3 < 2) hi = tr_read(wb + s3 * 320 + 160 + bo * 8);
              wf[bo][s3] = v4u{lo.x, lo.y, hi.x, hi.y};
            }
        }
        lds_wait(L, C_KV1, 8);
        if (n >= 2) {
#pragma unroll
          for (int m = 0; m < 4; ++m) {
            LAS char* dst = L + L_V + (hl * 128 + 64 * mh + 16 * m + tok) * 48;
            *(LAS v4u*)(dst) = c01[m];
            *(LAS v4u*)(dst + 16) = c23[m];
            *(LAS v4u*)(dst + 32) = v4u{c4[m].x, c4[m].y, 0x00003f80u, 0u};
          }
#pragma unroll
          for (int mm = 0; mm < 2; ++mm) {
            c01[mm] = *(LAS v4u*)(xch + mm * 2560 + lane * 16);
            c23[mm] = *(LAS v4u*)(xch + mm * 2560 + 1024 + lane * 16);
            c4[mm] = *(LAS v2u*)(xch + mm * 2560 + 2048 + lane * 8);
          }
        }
        {
          LAS char* dst0 = L + L_PK + (hl * 128 + 64 * mh + (n >= 2 ? 32 : 0) + tok) * 80;
          f32x4 pk[2][5];
#pragma unroll
          for (int m = 0; m < 2; ++m) {
            const v4u kf[3] = {c01[m], c23[m], v4u{c4[m].x, c4[m].y, 0u, 0u}};
#pragma unroll
            for (int bo = 0; bo < 5; ++bo) {
              pk[m][bo] = f32x4{0.f, 0.f, 0.f, 0.f};
#pragma unroll
              for (int s3 = 0; s3 < 3; ++s3) pk[m][bo] = mfma(wf[bo][s3], kf[s3], pk[m][bo]);
            }
          }
#pragma unroll
          for (int m = 0; m < 2; ++m) {
            float mx = pk[m][0][0], mn = pk[m][0][0];
#pragma unroll
            for (int bo = 0; bo < 5; ++bo)
#pragma unroll
              for (int r = 0; r < 4; ++r) { mx = fmaxf(mx, pk[m][bo][r]); mn = fminf(mn, pk[m][bo][r]); }
            const float l2e = 1.4426950408889634f;
            const float mxs = -mx * l2e, mns = mn * l2e;
            f32x2 ep[10], en[10];
#pragma unroll
            for (int i = 0; i < 10; ++i) {
              const f32x2 v = f32x2{pk[m][i >> 1][2 * (i & 1)], pk[m][i >> 1][2 * (i & 1) + 1]};
              ep[i] = v * l2e + mxs;
              en[i] = v * -l2e + mns;
            }
#pragma unroll
            for (int i = 0; i < 10; ++i) { ep[i] = exp2v(ep[i]); en[i] = exp2v(en[i]); }
            const f32x2 sp = (((ep[0] + ep[1]) + (ep[2] + ep[3])) + ((ep[4] + ep[5]) + (ep[6] + ep[7]))) + (ep[8] + ep[9]);
            const f32x2 sn = (((en[0] + en[1]) + (en[2] + en[3])) + ((en[4] + en[5]) + (en[6] + en[7]))) + (en[8] + en[9]);
            const float rp = __builtin_amdgcn_rcpf(sp[0] + sp[1]), rn = __builtin_amdgcn_rcpf(sn[0] + sn[1]);
            unsigned o[20];
#pragma unroll
            for (int i = 0; i < 10; ++i) { o[i] = cvtpk(ep[i] * rp); o[10 + i] = cvtpk(en[i] * rn); }
            LAS char* dst = dst0 + m * 16 * 80;
#pragma unroll
            for (int i = 0; i < 5; ++i) *(LAS v4u*)(dst + i * 16) = v4u{o[4 * i], o[4 * i + 1], o[4 * i + 2], o[4 * i + 3]};
          }
        }
        lds_arrive(L, C_KV2);
        lds_wait(L, C_KV2, 8);
        {
          f32x4 kv[3][2];
#pragma unroll
          for (int fb = 0; fb < 3; ++fb) { kv[fb][0] = f32x4{0.f, 0.f, 0.f, 0.f}; kv[fb][1] = f32x4{0.f, 0.f, 0.f, 0.f}; }
          LAS char* pa = L + L_PK + (w * 128 + 8 * g + q) * 80 + pp * 8;
          LAS char* pb = L + L_V + (w * 128 + 8 * g + q) * 48 + pp * 8;
#pragma unroll
          for (int ks = 0; ks < 4; ++ks) {
            v4u af[3], bf[2];
#pragma unroll
            for (int fb = 0; fb < 3; ++fb) af[fb] = tr_read2(pa + ks * 32 * 80 + fb * 32, 4 * 80);
#pragma unroll
            for (int vb = 0; vb < 2; ++vb) bf[vb] = tr_read2(pb + ks * 32 * 48 + vb * 32, 4 * 48);
#pragma unroll
            for (int fb = 0; fb < 3; ++fb)
#pragma unroll
              for (int vb = 0; vb < 2; ++vb) kv[fb][vb] = mfma(af[fb], bf[vb], kv[fb][vb]);
          }
          const int head = 8 * (cs - 11) + w;
          const unsigned rec = KVP_OFF + (head * 16 + rt) * KVP_REC;
          const unsigned o4 = rec + tok * 64 + g * 16, o2 = g < 2 ? rec + tok * 32 + g * 16 : 0x80000000u;
          const unsigned t5 = tok < 5 ? 0u : 0x80000000u;
          st16(rws, o4, __builtin_bit_cast(v4u, kv[0][0]));
          st16(rws, o4 + 1024, __builtin_bit_cast(v4u, kv[1][0]));
          st16(rws, o2 + 2048, __builtin_bit_cast(v4u, kv[2][0]));
          st16(rws, (o4 | t5) + 2560, __builtin_bit_cast(v4u, kv[0][1]));
          st16(rws, (o4 | t5) + 2880, __builtin_bit_cast(v4u, kv[1][1]));
          st16(rws, (o2 | t5) + 3200, __builtin_bit_cast(v4u, kv[2][1]));
            }
      }
    }
  }
  STAMP_AT(0, 2);

  const v2u tagv = *(LAS v2u*)(L + L_CTL + C_TAG);
  const v2u tag = v2u{rfl(tagv.x), rfl(tagv.y)};
  const int rt64 = (xcd >> 1) * 8 + (idx & 7), ct = (xcd & 1) * 4 + (idx >> 3);
  const __amdgpu_buffer_rsrc_t rw = mk_rsrc(ws + WT_OFF + ct * (NKS * 5120), NKH * 5120);
  const f32x4 z4 = {0.f, 0.f, 0.f, 0.f};

  if (w < 8) {
    asm volatile("s_waitcnt vmcnt(0) lgkmcnt(0)" ::: "memory");
    const int lane = fresh_lane();
    unsigned old = 0;
    if (lane == 0) old = __hip_atomic_fetch_add((LAS unsigned*)(L + L_CTL + C_DRAIN), 1u, __ATOMIC_RELAXED, __HIP_MEMORY_SCOPE_WORKGROUP);
    if (rfl(old) == 7u) {
      unsigned off = 0x80000000u;
      if (lane == 0) off = FL_ALL + bid * 8;
      else if (isGU) { if (lane == 1) off = FL_RT + rt * 128 + cs * 8; }
      else if (isAUX) { if (lane <= 8) off = FL_AUX + (lane - 1) * 128 + rt * 8; }
      else if (isKV) { if (lane <= 2) off = FL_KV + ((cs - 11) * 2 + lane - 1) * 128 + rt * 8; }
      else if (lane <= 8) off = FL_Q + (lane - 1) * 256 + (rt * 2 + cs - 9) * 8;
      else if (lane == 9) off = FL_RT + rt * 128 + cs * 8;
      st8(rws, off, tag);
    }
  }

  if (w >= 8) {
    LANE_VARS(fresh_lane());
    const unsigned vo = lane * 16;
    const int lw = w - 8;
    __builtin_amdgcn_s_setprio(1);
    if (lw == 0) {
      if (isAUX) lds_wait(L, C_DRAIN, 8);
      poll_line(rws, FL_AUX + xcd * 128, 0xffffu, tag);
      lds_arrive(L, C_AUXOK);
    }
    const int head = xcd * 4 + (idx & 3), rg = idx >> 2, t = lw * 64 + lane;
    v4u wa0, wa1;
    {
      const u16* wp = p.wphi + head * 400;
      unsigned e0[5], e1[5];
#pragma unroll
      for (int j = 0; j < 5; ++j) {
        const int d = j < 4 ? 4 * g + j : 16 + g;
        e0[j] = wp[d * 20 + tok];
        e1[j] = wp[d * 20 + 16 + (tok & 3)];
      }
      wa0 = v4u{e0[0] | e0[1] << 16, e0[2] | e0[3] << 16, e0[4], 0u};
      wa1 = v4u{e1[0] | e1[1] << 16, e1[2] | e1[3] << 16, e1[4], 0u};
    }
    if (isAUX) lds_wait(L, C_DRAIN, 8);
    if (lw == 1) { poll_line(rws, FL_Q + xcd * 256, (0x5u << (4 * rg + (head >> 4))) | (0x5u << (4 * rg)), tag); lds_arrive(L, C_T1); }
    else lds_wait(L, C_T1, 1);
    STAMP_AT(1, 3);
    v4u qf[4];
    {
      const unsigned qs = Q_OFF + (head * 32 + rg * 4 + lw) * 2560;
      const v4u q0 = __builtin_amdgcn_raw_buffer_load_b128(rws, vo, qs, 16), q1 = __builtin_amdgcn_raw_buffer_load_b128(rws, vo + 1024, qs, 16);
      const v2u q4 = __builtin_amdgcn_raw_buffer_load_b64(rws, lane * 8 + 2048, qs, 16);
      qf[0] = v4u{q0.x, q0.y, q4.x & 0xffffu, 0u};
      qf[1] = v4u{q0.z, q0.w, q4.x >> 16, 0u};
      qf[2] = v4u{q1.x, q1.y, q4.y & 0xffffu, 0u};
      qf[3] = v4u{q1.z, q1.w, q4.y >> 16, 0u};
    }
    const float gl = u2f(__builtin_amdgcn_raw_buffer_load_b32(rws, lane * 4, G_OFF + (rg * 256 + lw * 64) * 4, 16));
    v4u fp[4], fn[4];
#pragma unroll
    for (int m = 0; m < 4; ++m) {
      const f32x4 p0 = mfma(wa0, qf[m], z4);
      f32x4 p1 = mfma(wa1, qf[m], z4);
      if (g != 0) p1 = p0;
      float mx = fmaxf(fmaxf(fmaxf(p0[0], p0[1]), fmaxf(p0[2], p0[3])), fmaxf(fmaxf(p1[0], p1[1]), fmaxf(p1[2], p1[3])));
      float mn = fminf(fminf(fminf(p0[0], p0[1]), fminf(p0[2], p0[3])), fminf(fminf(p1[0], p1[1]), fminf(p1[2], p1[3])));
      const float l2e = 1.4426950408889634f;
      const float mxs = red4_max(mx) * -l2e, mns = red4_max(-mn) * -l2e;
      const float keep = g == 0 ? 1.0f : 0.0f;
      f32x2 ep[4], en[4];
#pragma unroll
      for (int i = 0; i < 4; ++i) {
        const f32x4 s = i < 2 ? p0 : p1;
        const f32x2 x = f32x2{s[2 * (i & 1)], s[2 * (i & 1) + 1]};
        ep[i] = exp2v(x * l2e + mxs);
        en[i] = exp2v(x * -l2e + mns);
      }
      const f32x2 sp = (ep[0] + ep[1]) + (ep[2] + ep[3]) * keep, sn = (en[0] + en[1]) + (en[2] + en[3]) * keep;
      const float rp = __builtin_amdgcn_rcpf(red4_sum(sp[0] + sp[1])), rn = __builtin_amdgcn_rcpf(red4_sum(sn[0] + sn[1]));
      const float rpk = rp * keep, rnk = rn * keep;
      fp[m] = v4u{cvtpk(ep[0] * rp), cvtpk(ep[1] * rp), cvtpk(ep[2] * rpk), cvtpk(ep[3] * rpk)};
      fn[m] = v4u{cvtpk(en[0] * rn), cvtpk(en[1] * rn), cvtpk(en[2] * rnk), cvtpk(en[3] * rnk)};
    }
    SB0();
    __builtin_amdgcn_s_setprio(OPRIO);
    STAMP_AT(1, 4);
    if (lw == 0) { poll_line(rws, FL_KV + xcd * 128, 0xffffu, tag); lds_arrive(L, C_KVOK); }
    else lds_wait(L, C_KVOK, 1);
    STAMP_AT(1, 5);
    {
      f32x4 v[16];
#pragma unroll
      for (int r = 0; r < 16; ++r)
        v[r] = __builtin_bit_cast(f32x4, __builtin_amdgcn_raw_buffer_load_b128(rws, t * 16, KVP_OFF + (head * 16 + r) * KVP_REC, 16));
      f32x4 s = v[0];
#pragma unroll
      for (int r = 1; r < 16; ++r) s += v[r];
      int d, f0;
      if (t < 128) { d = (t & 63) >> 2; f0 = 16 * (t >> 6) + 4 * (t & 3); }
      else if (t < 160) { d = (t - 128) >> 1; f0 = 32 + 4 * (t & 1); }
      else if (t < 200) { const int u = t < 180 ? t - 160 : t - 180; d = 16 + (u >> 2); f0 = (t < 180 ? 0 : 16) + 4 * (u & 3); }
      else { d = 16 + ((t - 200) >> 1); f0 = 32 + 4 * (t & 1); }
      if (t < 210) *(LAS v2u*)(L + L_KVS + d * 80 + f0 * 2) = cvt4(s);
    }
    lds_arrive(L, C_OSYNC);
    lds_wait(L, C_OSYNC, 4);
    STAMP_AT(1, 6);
    {
      v4u ka[2][2];
#pragma unroll
      for (int b = 0; b < 2; ++b)
#pragma unroll
        for (int st = 0; st < 2; ++st) {
          const int d = b == 0 ? tok : (tok < 4 ? 16 + tok : 20);
          LAS char* src = L + L_KVS + d * 80 + st * 40;
          const v2u lo = *(LAS v2u*)(src + g * 8), hi = *(LAS v2u*)(src + 32);
          ka[b][st] = v4u{lo.x, lo.y, hi.x, hi.y};
        }
      const int rto = rg * 4 + lw;
      const int Q0 = 5 * head + g, Q1 = 5 * head + 4;
      const unsigned o0 = (unsigned)(ACT_OFF + (rto * NKS + NKH + (Q0 >> 3)) * 4096 + (Q0 & 7) * 256 + tok * 16);
      const unsigned o1 = g == 0 ? (unsigned)(ACT_OFF + (rto * NKS + NKH + (Q1 >> 3)) * 4096 + (Q1 & 7) * 256 + tok * 16) : 0x80000000u;
      v2u a[4], b[4];
#pragma unroll
      for (int m = 0; m < 4; ++m) {
        f32x4 n0 = mfma(ka[0][0], fp[m], z4), n1 = mfma(ka[1][0], fp[m], z4);
        n0 = mfma(ka[0][1], fn[m], n0);
        n1 = mfma(ka[1][1], fn[m], n1);
        auto r = __builtin_amdgcn_permlane16_swap(f2u(n1[0]), f2u(n1[0]), false, false);
        const unsigned r1 = r[1];
        const float gm = u2f(__builtin_amdgcn_ds_bpermute((m * 16 + tok) * 4, f2u(gl)));
        const float sc = gm * __builtin_amdgcn_rcpf(u2f(r1) + 1e-6f);
        a[m] = cvt4(n0 * sc); b[m] = cvt4(n1 * sc);
        if (m & 1) {
          st16(rws, o0 + (m >> 1) * 2048, v4u{a[m - 1].x, a[m - 1].y, a[m].x, a[m].y});
          st16(rws, o1 + (m >> 1) * 2048, v4u{b[m - 1].x, b[m - 1].y, b[m].x, b[m].y});
        }
      }
    }
    asm volatile("s_waitcnt vmcnt(0)" ::: "memory");
    if (lane == 0) st8(rws, FL_O + (rg * 4 + lw) * 256 + head * 8, tag);
    STAMP_AT(1, 7);
    if (lw == 1) { poll_line(rws, FL_O + rt64 * 256, 0xffffffffu, tag); lds_arrive(L, C_OOK); }
    if (bid == 0 && lw == 0) {
      for (unsigned spins = 0; spins < (1u << 18); ++spins) {
        bool ok = true;
#pragma unroll
        for (int i = 0; i < 2; ++i) {
          const v4u v = __builtin_amdgcn_raw_buffer_load_b128(rws, vo + i * 1024, FL_ALL, 16);
          ok &= v.x == tag.x && v.y == tag.y && v.z == tag.x && v.w == tag.y;
        }
        if (__all(ok)) break;
        __builtin_amdgcn_s_sleep(POLL_SLEEP);
      }
      if (lane == 0) st8(rws, FL_EPOCH, tag);
    }
    STAMP_FLUSH(1);
    return;
  }

  __builtin_amdgcn_s_setprio(0);
  STAMP_AT(0, 3);

  const __amdgpu_buffer_rsrc_t ra = mk_rsrc(ws + ACT_OFF + rt64 * (NKS * 4096), NKH * 4096);
  const __amdgpu_buffer_rsrc_t rao = mk_rsrc(ws + ACT_OFF + (rt64 * NKS + NKH) * 4096, (NKS - NKH) * 4096);
  const __amdgpu_buffer_rsrc_t rwo = mk_rsrc(ws + WT_OFF + (ct * NKS + NKH) * 5120, (NKS - NKH) * 5120);
  const int h0 = 7 * w, o0 = 3 * (7 - w);
  const unsigned vo = fresh_lane() * 16;
  Stage st[2];
  if (w == 7) {
    __builtin_amdgcn_s_setprio(3);
    const unsigned fo = (vo >> 1) & 127;
    for (unsigned spins = 0; spins < (1u << 18); ++spins) {
      const v2u v = __builtin_amdgcn_raw_buffer_load_b64(rws, fo, FL_RT + (rt64 >> 1) * 128, 16);
      const unsigned ready = (unsigned)__builtin_amdgcn_ballot_w64(v.x == tag.x && v.y == tag.y) & 0x3ffu;
      *(volatile LAS unsigned*)(L + L_CTL + C_HOK) = ready;
      if (ready == 0x3ffu) break;
      __builtin_amdgcn_s_sleep(POLL_SLEEP);
    }
    __builtin_amdgcn_s_setprio(0);
  }
  f32x4 acc[5][4];
  lds_wait(L, C_AUXOK, 1);
  {
    const unsigned need = (3u << (h0 / 6)) | 0x200u;
    volatile LAS unsigned* f = (volatile LAS unsigned*)(L + L_CTL + C_HOK);
    for (unsigned spins = 0; (rfl(*f) & need) != need && spins < (1u << 22); ++spins) __builtin_amdgcn_s_sleep(1);
    asm volatile("" ::: "memory");
  }
  STAMP_AT(0, 5);
  p2_load_b(st[0], ra, vo, h0);
  p2_load_a(st[0], rw, vo, h0);
  p2_load_b(st[1], ra, vo, h0 + 1);
  p2_load_a(st[1], rw, vo, h0 + 1);
  float gl = 0.f;
  SB0();
#pragma unroll
  for (int i = 0; i < 10; ++i) {
    Stage& cur = st[i & 1];
    if (i < 7) {
#pragma unroll
      for (int nb = 0; nb < 5; ++nb) {
#pragma unroll
        for (int m = 0; m < 4; ++m) {
          if (i == 0) mfma_init(acc[nb][m], cur.a[nb], cur.b[m]);
          else mfma_ip2(acc[nb][m], cur.a[nb], cur.b[m]);
        }
      }
      if (i == 6) {
        SB0();
        asm volatile("s_nop 15" ::: "memory");
        SB0();
        const int tk4 = (fresh_lane() & 15) * 4;
#pragma unroll
        for (int m = 0; m < 4; ++m) {
          const float gh = 1.0f - u2f(__builtin_amdgcn_ds_bpermute(m * 64 + tk4, f2u(gl)));
#pragma unroll
          for (int nb = 0; nb < 5; ++nb) acc[nb][m] *= gh;
        }
        SB0();
      }
    } else {
#pragma unroll
      for (int pr = 0; pr < 2; ++pr) {
        const unsigned tx = cur.b[2 * pr].z, ty = cur.b[2 * pr].w;
        cur.b[2 * pr].z = cur.b[2 * pr + 1].x; cur.b[2 * pr].w = cur.b[2 * pr + 1].y;
        cur.b[2 * pr + 1].x = tx; cur.b[2 * pr + 1].y = ty;
      }
#pragma unroll
      for (int nb = 0; nb < 5; ++nb)
#pragma unroll
        for (int m = 0; m < 4; ++m) mfma_ip2(acc[nb][m], cur.a[nb], cur.b[m]);
    }
    SB0();
    const int nx = i + 2;
    if (nx == 6) gl = u2f(__builtin_amdgcn_raw_buffer_load_b32(rws, vo >> 2, G_OFF + rt64 * 256, 16));
    if (nx < 7) { p2_load_b(cur, ra, vo, h0 + nx); p2_load_a(cur, rw, vo, h0 + nx); }
    else if (nx < 10) {
      p2_load_a(cur, rwo, vo, o0 + nx - 7);
      if (nx == 7) lds_wait(L, C_OOK, 1);
      p2_load_b(cur, rao, vo, o0 + nx - 7);
    }
    SB0();
  }
  STAMP_AT(0, 6);
  asm volatile("s_nop 15" ::: "memory");
  lds_wait(L, C_DRAIN, 8);
  SB0();
#pragma unroll
  for (int m = 0; m < 3; ++m)
#pragma unroll
    for (int nb = 0; nb < 5; ++nb) *(LAS f32x4*)(L + ((w * 4 + m) * 5 + nb) * 1024 + vo) = acc[nb][m];
  if (w < 7) {
#pragma unroll
    for (int nb = 0; nb < 5; ++nb) *(LAS f32x4*)(L + ((w * 4 + 3) * 5 + nb) * 1024 + vo) = acc[nb][3];
  }
  SYNCL();
  {
    LANE_VARS(fresh_lane());
    const __amdgpu_buffer_rsrc_t ro_ = mk_rsrc(p.out, S_ * D_ * 2);
    const unsigned op = (unsigned)(((rt64 * 64 + tok) * D_ + ct * 80) * 2);
    if (w < 6) {
      const int m = w % 3, hf = w / 3;
      LAS char* src = L + (m * 5 + hf * 2) * 1024 + vo;
      f32x4 r0 = *(LAS f32x4*)(src), r1 = *(LAS f32x4*)(src + 1024);
#pragma unroll
      for (int sw = 1; sw < 8; ++sw) { r0 += *(LAS f32x4*)(src + sw * 20480); r1 += *(LAS f32x4*)(src + sw * 20480 + 1024); }
      __builtin_amdgcn_raw_buffer_store_b128(cvt8(r0, r1), ro_, op + m * 16 * D_ * 2 + hf * 64 + 16 * g, 0, 16);
    } else if (w == 6) {
#pragma unroll
      for (int m = 0; m < 3; ++m) {
        LAS char* src = L + (m * 5 + 4) * 1024 + vo;
        f32x4 r = *(LAS f32x4*)(src);
#pragma unroll
        for (int sw = 1; sw < 8; ++sw) r += *(LAS f32x4*)(src + sw * 20480);
        __builtin_amdgcn_raw_buffer_store_b64(cvt4(r), ro_, op + m * 16 * D_ * 2 + 128 + 8 * g, 0, 16);
      }
    } else {
#pragma unroll
      for (int sw = 0; sw < 7; ++sw)
#pragma unroll
        for (int nb = 0; nb < 5; ++nb) acc[nb][3] += *(LAS f32x4*)(L + ((sw * 4 + 3) * 5 + nb) * 1024 + vo);
      __builtin_amdgcn_raw_buffer_store_b128(cvt8(acc[0][3], acc[1][3]), ro_, op + 3 * 16 * D_ * 2 + 16 * g, 0, 16);
      __builtin_amdgcn_raw_buffer_store_b128(cvt8(acc[2][3], acc[3][3]), ro_, op + 3 * 16 * D_ * 2 + 64 + 16 * g, 0, 16);
      __builtin_amdgcn_raw_buffer_store_b64(cvt4(acc[4][3]), ro_, op + 3 * 16 * D_ * 2 + 128 + 8 * g, 0, 16);
    }
  }
  STAMP_AT(0, 7);
  STAMP_FLUSH(0);
}

extern "C" void kernel_launch(void* const* d_in, const int* in_sizes, int n_in, void* d_out, int out_size, void* d_ws,
                              size_t ws_size, hipStream_t stream) {
  Args a;
  a.x = (const u16*)d_in[0]; a.wqkv = (const u16*)d_in[1]; a.wphi = (const u16*)d_in[2]; a.wo = (const u16*)d_in[3];
  a.wg = (const u16*)d_in[5]; a.wu = (const u16*)d_in[6]; a.wd = (const u16*)d_in[7]; a.gw = (const u16*)d_in[8];
  a.ws = (char*)d_ws;
  a.out = (u16*)d_out;
  (void)hipFuncSetAttribute((const void*)fused, hipFuncAttributeMaxDynamicSharedMemorySize, K1_LDS);
  fused<<<256, 768, K1_LDS, stream>>>(a);
}
```

```cpp
#include <hip/hip_runtime.h>

typedef unsigned short u16;
typedef unsigned v4u __attribute__((ext_vector_type(4)));
typedef unsigned v2u __attribute__((ext_vector_type(2)));
typedef float f32x4 __attribute__((ext_vector_type(4)));
typedef float f32x2 __attribute__((ext_vector_type(2)));
typedef short s16x4 __attribute__((ext_vector_type(4)));
typedef __bf16 bf16x8 __attribute__((ext_vector_type(8)));
typedef __bf16 bf16x2 __attribute__((ext_vector_type(2)));
#define LAS __attribute__((address_space(3)))
#define DI __device__ __forceinline__

constexpr int S_ = 2048, D_ = 640, F_ = 1728;
constexpr int NKS = 74, NKH = 54;
constexpr unsigned ACT_OFF = 0, ACT_BYTES = 32u * NKS * 4 * 1024;
constexpr unsigned WT_OFF = ACT_OFF + ACT_BYTES, WT_BYTES = 8u * NKS * 5 * 1024;
constexpr unsigned Q_OFF = WT_OFF + WT_BYTES, Q_BYTES = 32u * 128 * 1024;
constexpr unsigned KVP_REC = 1792;
constexpr unsigned KVP_OFF = Q_OFF + Q_BYTES, KVP_BYTES = 32u * 16 * KVP_REC;
constexpr unsigned G_OFF = KVP_OFF + KVP_BYTES, G_BYTES = S_ * 4;
constexpr unsigned FS = 128;
constexpr unsigned FL_EPOCH = G_OFF + G_BYTES + 65536, FL_ALL = FL_EPOCH + 256, FL_RT = FL_ALL + 2048, FL_AUX = FL_RT + 16 * 16 * FS,
                   FL_KV = FL_AUX + 8 * 16 * FS, FL_Q = FL_KV + 8 * 16 * FS, FL_O = FL_Q + 8 * 32 * FS;
constexpr unsigned WS_BYTES = FL_O + 32 * 32 * FS;

constexpr unsigned L_W = 0, WSLOT = 24576, L_X = 4 * WSLOT, XSLOT = 16384, L_WPHI = L_X + 2 * XSLOT, L_ZERO = L_WPHI + 6400,
                   L_DUMMY = L_WPHI + 8192, K1_LDS = 163840;
constexpr unsigned L_KVS = 161792, L_CTL = 163584;
enum { C_TAG = 0, C_DRAIN = 76, C_X = 16, C_KV1 = 32, C_KV2 = 36, C_AUXOK = 40, C_PRE = 44, C_CONS = 48, C_OSYNC = 52, C_KVOK = 56, C_T1 = 60, C_HOK = 64, C_T2 = 68, C_OOK = 72 };
constexpr unsigned L_PK = 0, L_V = 8 * 128 * 80;

DI int fresh_lane() {
  int l;
  asm volatile("v_mbcnt_lo_u32_b32 %0, -1, 0\n\tv_mbcnt_hi_u32_b32 %0, -1, %0" : "=v"(l));
  return l;
}
#define LANE_VARS(l) const int lane = (l), g = lane >> 4, q = (lane >> 2) & 3, pp = lane & 3, tok = lane & 15; (void)q; (void)pp; (void)tok; (void)g
DI unsigned rfl(unsigned x) { return __builtin_amdgcn_readfirstlane(x); }
DI unsigned cvtpk(f32x2 v) {
  bf16x2 b = __builtin_convertvector(v, bf16x2);
  return __builtin_bit_cast(unsigned, b);
}
DI unsigned cvtpk(float lo, float hi) { return cvtpk(f32x2{lo, hi}); }
DI v2u cvt4(f32x4 v) { return v2u{cvtpk(v[0], v[1]), cvtpk(v[2], v[3])}; }
DI v4u cvt8(f32x4 a, f32x4 b) { return v4u{cvtpk(a[0], a[1]), cvtpk(a[2], a[3]), cvtpk(b[0], b[1]), cvtpk(b[2], b[3])}; }
DI float bf2f(unsigned b) { return __builtin_bit_cast(float, b << 16); }
DI f32x4 mfma(v4u a, v4u b, f32x4 c) {
  return __builtin_amdgcn_mfma_f32_16x16x32_bf16(__builtin_bit_cast(bf16x8, a), __builtin_bit_cast(bf16x8, b), c, 0, 0, 0);
}
DI v2u tr_read(LAS char* p) {
  s16x4 r = __builtin_amdgcn_ds_read_tr16_b64_v4i16((LAS s16x4*)p);
  return __builtin_bit_cast(v2u, r);
}
DI v4u tr_read2(LAS char* p, int d) {
  v2u a = tr_read(p), b = tr_read(p + d);
  return v4u{a.x, a.y, b.x, b.y};
}
DI __amdgpu_buffer_rsrc_t mk_rsrc(const void* p, unsigned bytes) {
  return __builtin_amdgcn_make_buffer_rsrc((void*)p, (short)0, (int)bytes, 0x00020000);
}
DI float u2f(unsigned u) { return __builtin_bit_cast(float, u); }
DI unsigned f2u(float f) { return __builtin_bit_cast(unsigned, f); }
DI float red4_max(float x) {
  auto r = __builtin_amdgcn_permlane16_swap(f2u(x), f2u(x), false, false);
  unsigned r0 = r[0], r1 = r[1];
  x = fmaxf(u2f(r0), u2f(r1));
  auto t = __builtin_amdgcn_permlane32_swap(f2u(x), f2u(x), false, false);
  r0 = t[0]; r1 = t[1];
  return fmaxf(u2f(r0), u2f(r1));
}
DI float red4_sum(float x) {
  auto r = __builtin_amdgcn_permlane16_swap(f2u(x), f2u(x), false, false);
  unsigned r0 = r[0], r1 = r[1];
  x = u2f(r0) + u2f(r1);
  auto t = __builtin_amdgcn_permlane32_swap(f2u(x), f2u(x), false, false);
  r0 = t[0]; r1 = t[1];
  return u2f(r0) + u2f(r1);
}
DI f32x2 exp2v(f32x2 a) { return f32x2{__builtin_amdgcn_exp2f(a[0]), __builtin_amdgcn_exp2f(a[1])}; }
DI void st16(__amdgpu_buffer_rsrc_t r, unsigned off, v4u v) { __builtin_amdgcn_raw_buffer_store_b128(v, r, off, 0, 16); }
DI void st8(__amdgpu_buffer_rsrc_t r, unsigned off, v2u v) { __builtin_amdgcn_raw_buffer_store_b64(v, r, off, 0, 16); }
#define SB0() __builtin_amdgcn_sched_barrier(0)
#define SYNC(n) do { SB0(); asm volatile("s_waitcnt vmcnt(" #n ") lgkmcnt(0)\n\ts_barrier" ::: "memory"); SB0(); } while (0)
#define LDSDMA(m0v, vo, rs, so) \
  asm volatile("s_mov_b32 m0, %0\n\ts_nop 0\n\tbuffer_load_dwordx4 %1, %2, %3 offen nt lds" :: "s"(m0v), "v"(vo), "s"(rs), "s"(so) : "memory")

struct Args {
  const u16 *x, *wqkv, *wphi, *wo, *wg, *wu, *wd, *gw;
  char* ws;
  u16* out;
};

#define SYNCL() do { SB0(); asm volatile("s_waitcnt lgkmcnt(0)\n\ts_barrier" ::: "memory"); SB0(); } while (0)
#define DMA(m0v, vo, rs, so, off) \
  asm volatile("s_mov_b32 m0, %0\n\ts_nop 0\n\tbuffer_load_dwordx4 %1, %2, %3 offen offset:%4 lds" :: "s"(m0v), "v"(vo), "s"(rs), "s"(so), "i"(off) : "memory")
#define DMAS(m0v, vo, rs, so, off) \
  asm volatile("s_mov_b32 m0, %0\n\ts_nop 0\n\tbuffer_load_dwordx4 %1, %2, %3 offen offset:%4 sc1 lds" :: "s"(m0v), "v"(vo), "s"(rs), "s"(so), "i"(off) : "memory")

template <int MODE> struct MI { static constexpr int NB = MODE == 1 ? 5 : 6; };
template <int MODE, int B, int R, int SLOT> DI v2u a_read(LAS char* L, unsigned (&aoff)[6]) {
  constexpr int I = MODE == 0 ? B >> 1 : B;
  if constexpr (MODE == 2 && B == 5) {
    const v2u t = *(LAS v2u*)(L + aoff[5] + SLOT * 64 + R * 8);
    if constexpr (R == 1 && SLOT == 3) asm volatile("v_add_u32 %0, 0x100, %0" : "+v"(aoff[5]));
    return t;
  }
  const v2u t = tr_read(L + L_W + (SLOT & 1) * WSLOT + aoff[I] + ((MODE == 0 && (B & 1)) ? 12288 : 0) + R * 1536);
  if constexpr (R == 1 && (MODE != 0 || (B & 1))) {
    if constexpr (SLOT == 1) asm volatile("v_add_u32 %0, 0xc000, %0" : "+v"(aoff[I]));
    if constexpr (SLOT == 3) asm volatile("v_subrev_u32 %0, 0xc000, %0" : "+v"(aoff[I]));
  }
  return t;
}
DI void mfma_ip(f32x4& c, const v4u& a, const v4u& b) {
  asm volatile("v_mfma_f32_16x16x32_bf16 %0, %1, %2, %0" : "+v"(c) : "v"(a), "v"(b));
}
template <int MODE, int T, int J = 0>
DI void k1_step(f32x4 (&acc)[4][MI<MODE>::NB], v4u (&a)[4], v4u (&x)[2][4], LAS char* L, unsigned (&aoff)[6],
                const unsigned (&xoff)[2]) {
  constexpr int NB = MI<MODE>::NB, R0 = T * NB;
  if constexpr (J < 4 * NB) {
    constexpr int b = J >> 2, r = J & 3, nb = b + 2;
    if constexpr (r < 2) {
      v2u t = a_read<MODE, nb % NB, r, (nb < NB ? T : T + 1) & 3>(L, aoff);
      if constexpr (r == 0) { a[(R0 + nb) & 3].x = t.x; a[(R0 + nb) & 3].y = t.y; }
      else { a[(R0 + nb) & 3].z = t.x; a[(R0 + nb) & 3].w = t.y; }
    } else if constexpr (r == 2 && b < 4)
      x[(T + 1) & 1][b] = *(LAS v4u*)(L + xoff[(T + 1) & 1] + ((((T + 1) >> 1) & 1) * XSLOT + b * 2048));
    mfma_ip(acc[r][b], a[(R0 + b) & 3], x[T & 1][r]);
    SB0();
    k1_step<MODE, T, J + 1>(acc, a, x, L, aoff, xoff);
  }
}
template <int MODE>
DI void k1_mainloop(f32x4 (&acc)[4][MI<MODE>::NB], LAS char* L, unsigned (&aoff)[6], unsigned xoff0) {
  unsigned xoff[2] = {xoff0 + L_X, (xoff0 ^ 64) + L_X};
  asm volatile("" : "+v"(xoff[0]), "+v"(xoff[1]));
  v4u a[4], x[2][4];
#pragma unroll
  for (int m = 0; m < 4; ++m)
#pragma unroll
    for (int b = 0; b < MI<MODE>::NB; ++b) {
      acc[m][b] = f32x4{0.f, 0.f, 0.f, 0.f};
    }
  SYNCL();
#pragma unroll
  for (int m = 0; m < 4; ++m) x[0][m] = *(LAS v4u*)(L + xoff[0] + m * 2048);
  {
    const v2u t0 = a_read<MODE, 0, 0, 0>(L, aoff), t1 = a_read<MODE, 0, 1, 0>(L, aoff);
    const v2u t2 = a_read<MODE, 1, 0, 0>(L, aoff), t3 = a_read<MODE, 1, 1, 0>(L, aoff);
    a[0] = v4u{t0.x, t0.y, t1.x, t1.y}; a[1] = v4u{t2.x, t2.y, t3.x, t3.y};
  }
#pragma unroll 1
  for (int i = 0; i < 20; i += 4) {
    SYNCL(); k1_step<MODE, 0>(acc, a, x, L, aoff, xoff);
    SYNCL(); k1_step<MODE, 1>(acc, a, x, L, aoff, xoff);
    SYNCL(); k1_step<MODE, 2>(acc, a, x, L, aoff, xoff);
    SYNC(0); k1_step<MODE, 3>(acc, a, x, L, aoff, xoff);
  }
  asm volatile("s_nop 15" ::: "memory");
  __builtin_amdgcn_s_setprio(2);
  SB0();
}

DI unsigned gelu_mul2(f32x2 x, f32x2 u) {
  const float k0 = -2.0f * 0.7978845608028654f * 1.4426950408889634f, k1 = k0 * 0.044715f;
  f32x2 t = x * x;
  f32x2 a = x * (t * k1 + k0);
  f32x2 d = exp2v(a) + 1.0f;
  f32x2 r = f32x2{__builtin_amdgcn_rcpf(d[0]), __builtin_amdgcn_rcpf(d[1])};
  return cvtpk((x * u) * r);
}
DI v2u gelu_mul4(f32x4 x, f32x4 u) {
  return v2u{gelu_mul2(f32x2{x[0], x[1]}, f32x2{u[0], u[1]}), gelu_mul2(f32x2{x[2], x[3]}, f32x2{u[2], u[3]})};
}

constexpr unsigned CODE_PF = 38656;
constexpr int POLL_SLEEP = 6;
DI void lds_arrive(LAS char*, unsigned word) {
  asm volatile("s_waitcnt lgkmcnt(0)\n\ts_mov_b64 exec, 1\n\tds_add_u32 %0, %1\n\ts_mov_b64 exec, -1" :: "v"(L_CTL + word), "v"(1u) : "memory");
}
DI void lds_wait(LAS char*, unsigned word, unsigned target) {
  unsigned v, t, n = 1u << 22;
  asm volatile("1:\n\tds_read_b32 %0, %3\n\ts_waitcnt lgkmcnt(0)\n\tv_readfirstlane_b32 %1, %0\n\ts_cmp_ge_u32 %1, %4\n\ts_cbranch_scc1 2f\n\t"
               "s_sleep 1\n\ts_sub_u32 %2, %2, 1\n\ts_cbranch_scc0 1b\n2:"
               : "=&v"(v), "=&s"(t), "+s"(n) : "v"(L_CTL + word), "s"(target) : "memory", "scc");
}
DI void lds_wait_bits(LAS char*, unsigned word, unsigned need) {
  unsigned v, t, n = 1u << 22;
  asm volatile("1:\n\tds_read_b32 %0, %3\n\ts_waitcnt lgkmcnt(0)\n\tv_readfirstlane_b32 %1, %0\n\ts_andn2_b32 %1, %4, %1\n\ts_cbranch_scc0 2f\n\t"
               "s_sleep 1\n\ts_sub_u32 %2, %2, 1\n\ts_cbranch_scc0 1b\n2:"
               : "=&v"(v), "=&s"(t), "+s"(n) : "v"(L_CTL + word), "s"(need) : "memory", "scc");
}
DI void poll(__amdgpu_buffer_rsrc_t rws, unsigned off, v2u tag, int n) {
  if (fresh_lane() >= n) off = 0x80000000u;
#pragma unroll 1
  for (unsigned spins = 0; spins < (1u << 18); ++spins) {
    const v2u v = __builtin_amdgcn_raw_buffer_load_b64(rws, off, 0, 16);
    if (v.x == tag.x && v.y == tag.y) off = 0x80000000u;
    if (!__any(off != 0x80000000u)) break;
    __builtin_amdgcn_s_sleep(POLL_SLEEP);
  }
  __builtin_amdgcn_fence(__ATOMIC_ACQUIRE, "wavefront");
}
DI void mfma_ip2(f32x4& c, const v4u& a, const v4u& b) {
  asm volatile("s_nop 1\n\tv_mfma_f32_16x16x32_bf16 %0, %1, %2, %0" : "+v"(c) : "v"(a), "v"(b));
}
DI void mfma_init(f32x4& c, const v4u& a, const v4u& b) {
  asm volatile("s_nop 1\n\tv_mfma_f32_16x16x32_bf16 %0, %1, %2, 0" : "=&v"(c) : "v"(a), "v"(b));
}
struct Stage { v4u a[5], b[4]; };
DI void p2_load_b(Stage& st, __amdgpu_buffer_rsrc_t ra, unsigned vo, int ks) {
#pragma unroll
  for (int i = 0; i < 4; ++i) st.b[i] = __builtin_amdgcn_raw_buffer_load_b128(ra, vo + i * 1024, ks * 4096, 16);
}
DI void p2_load_a(Stage& st, __amdgpu_buffer_rsrc_t rw, unsigned vo, int ks) {
#pragma unroll
  for (int nb = 0; nb < 5; ++nb) st.a[nb] = __builtin_amdgcn_raw_buffer_load_b128(rw, vo + nb * 1024, ks * 5120, 16);
}

__global__ __launch_bounds__(768, 3) void fused(Args p) {
  extern __shared__ __attribute__((aligned(16))) char smem[];
  LAS char* L = (LAS char*)smem;
  unsigned long long pc;
  asm volatile("s_getpc_b64 %0" : "=s"(pc));
  const int w = rfl(threadIdx.x >> 6);
  const int bid = blockIdx.x, xcd = bid & 7, idx = bid >> 3;
  const unsigned long long CSMAP = 0xDC87BA65E943F210ull;
  int rl = idx & 7, grp = xcd >> 1;
  if (idx >= 24) {
    if (grp == 0) { if (rl >= 2) { grp = 1 + ((rl - 2) >> 1); rl = 6 + (rl & 1); } }
    else if (rl >= 6) { rl = 2 * grp + (rl - 6); grp = 0; }
  }
  const int rt = (xcd & 1) * 8 + rl, cs = (int)(CSMAP >> (4 * (grp * 4 + (idx >> 3)))) & 15;
  char* ws = p.ws;
  const __amdgpu_buffer_rsrc_t rws = mk_rsrc(ws, WS_BYTES);
  const bool isAUX = cs == 15, isGU = cs < 9, isKV = cs >= 11 && cs < 15;

  asm volatile("" :: "s"(p.x), "s"(p.wqkv), "s"(p.wg), "s"(p.wu), "s"(p.ws));
  v2u hlook = v2u{0u, 0u};
  if (w >= 8 && !isAUX) {
    const __amdgpu_buffer_rsrc_t rx = mk_rsrc(p.x + rt * 128 * D_, (unsigned)((S_ - rt * 128) * D_ * 2));
      __builtin_amdgcn_s_setprio(3);
      const int lane = fresh_lane();
      const int lw = w - 8, sub = lw >> 1;
      const int r0 = 32 * lw + (lane >> 3);
      const unsigned vx0 = (unsigned)(r0 * 1280 + (((lane & 7) ^ ((r0 >> 1) & 7)) * 16)), vx1 = (vx0 ^ 64u) + 8 * 1280 - 1024;
      const unsigned m0x = L_X + lw * 4096;
#define LOAD_X(stage, slot, e) do { const unsigned m0_ = m0x + (unsigned)(slot) * XSLOT + (e) * 2048, so_ = (unsigned)(stage) * 128 + (e) * 16 * 1280; \
      DMA(m0_, vx0, rx, so_, 0); DMA(m0_, vx1, rx, so_, 1024); } while (0)
#define LOAD_XX(stage) do { LOAD_X(stage, (stage) & 1, 0); LOAD_X(stage, (stage) & 1, 1); } while (0)
      LOAD_XX(0);
      if (cs == 9 && lw < 2) {
        const __amdgpu_buffer_rsrc_t rg = mk_rsrc(p.gw, D_ * 2);
        DMA(L_WPHI + lw * 1024, (unsigned)(lw * 1024 + lane * 16), rg, 0, 0);
      }
    const unsigned pitch = isGU ? F_ * 2 : 3 * D_ * 2, total = D_ * pitch;
    unsigned boffA, boffB;
    if (isGU) boffA = boffB = 384u * cs;
    else if (!isKV) { boffA = 640u * (cs - 9); boffB = boffA + 256u; }
    else { const int j = cs - 11; boffA = 1280u + 320u * j - ((j & 1) ? 64u : 0u); boffB = boffA + 1280u; }
    const char* baseA = (const char*)(isGU ? p.wg : p.wqkv) + boffA;
    const char* baseB = (const char*)(isGU ? p.wu : p.wqkv) + boffB;
      const __amdgpu_buffer_rsrc_t rw = mk_rsrc((sub ? baseB : baseA) + (lw & 1) * 16 * pitch, total - (sub ? boffB : boffA) - (lw & 1) * 16 * pitch);
      unsigned vw[6];
#pragma unroll
      for (int j = 0; j < 3; ++j) {
        const int Lc = 64 * j + lane, kr = Lc / 24, pos = Lc % 24;
        const unsigned c0 = (unsigned)((pos >> 3) * 128 + (((pos & 7) ^ (kr & 2)) * 16));
        vw[j] = (unsigned)kr * pitch + c0 - 1024u * j;
        vw[3 + j] = (unsigned)(kr + 8) * pitch + (c0 ^ 64u) - 1024u * j;
      }
      const unsigned m0w = L_W + sub * 12288 + (lw & 1) * 6144, wstep = 32 * pitch;
#define LOAD_W(s) do { const unsigned m0_ = m0w + (unsigned)((s) & 3) * WSLOT, so_ = (unsigned)(s) * wstep; \
      DMA(m0_, vw[0], rw, so_, 0); DMA(m0_, vw[1], rw, so_, 1024); DMA(m0_, vw[2], rw, so_, 2048); \
      DMA(m0_ + 3072, vw[3], rw, so_, 0); DMA(m0_ + 3072, vw[4], rw, so_, 1024); DMA(m0_ + 3072, vw[5], rw, so_, 2048); } while (0)
      const __amdgpu_buffer_rsrc_t rpf = lw == 3 ? rx : mk_rsrc(((rt & 1) ? baseB : baseA) + 8 * (rt >> 1) * pitch, total - ((rt & 1) ? boffB : boffA) - 8 * (rt >> 1) * pitch);
      const unsigned vpf = lw == 3 ? (unsigned)((8 * cs + (lane >> 3)) * 1280 + (lane & 7) * 16) : lw == 0 ? vw[0] : lw == 1 ? vw[1] + 1024u : vw[2] + 2048u;
      const unsigned pfstep = lw == 3 ? 128u : 64u * pitch;
#define PF(stage) DMA(L_DUMMY + lw * 1024, vpf, rpf, (unsigned)(stage) * pfstep, 0)
#define WPHI() do { if (isKV) {   \
        const __amdgpu_buffer_rsrc_t rp = mk_rsrc(p.wphi + 8 * (cs - 11) * 400, 6400); \
        DMA(L_WPHI + lw * 2048, (unsigned)(lw * 2048 + lane * 16), rp, 0, 0); \
        DMA(L_WPHI + lw * 2048, (unsigned)(lw * 2048 + lane * 16), rp, 0, 1024); } } while (0)
      LOAD_W(0); LOAD_W(1); LOAD_W(2);
      SYNC(12); LOAD_XX(1); PF(2); PF(3);
      SYNC(12); LOAD_W(3); PF(4);
      SYNC(9); LOAD_XX(2); LOAD_W(4); PF(5);
      SYNC(12); LOAD_W(5); PF(6);
      SYNC(8); LOAD_XX(3); LOAD_W(6);
      SYNC(11); LOAD_W(7); PF(7);
      SYNC(7); LOAD_XX(4); LOAD_W(8);
      SYNC(11); LOAD_W(9); PF(8);
      SYNC(7); LOAD_XX(5); LOAD_W(10);
      SYNC(11); LOAD_W(11); PF(9);
      SYNC(7); LOAD_XX(6); LOAD_W(12);
      SYNC(11); LOAD_W(13);
      SYNC(6); LOAD_XX(7); LOAD_W(14);
      SYNC(10); LOAD_W(15);
      SYNC(6); LOAD_XX(8); LOAD_W(16);
      SYNC(10); LOAD_W(17);
      SYNC(6); LOAD_XX(9); LOAD_W(18);
      SYNC(10); LOAD_W(19); WPHI();
      SYNC(6);
      SYNC(0);
      SYNC(0);
  } else {
  if (w == 1) {
    const __amdgpu_buffer_rsrc_t rc = mk_rsrc((const void*)(pc & ~255ull), CODE_PF);
    DMA(L_DUMMY + 4096, (unsigned)(idx * 2048 + fresh_lane() * 16), rc, 0, 0);
    DMA(L_DUMMY + 4096, (unsigned)(idx * 2048 + fresh_lane() * 16), rc, 0, 1024);
  }
  if (w == 0) {
    const int lane = fresh_lane();
    if (lane >= 4 && lane < 32) *(LAS unsigned*)(L + L_CTL + lane * 4) = 0u;
    if (lane == 0) DMAS(L_CTL, 0u, rws, FL_EPOCH, 0);
  }

  if (isAUX) {
    LANE_VARS(fresh_lane());
    if (w >= 8) {
      SYNCL();
    } else {
      const __amdgpu_buffer_rsrc_t rd = mk_rsrc(p.wd, F_ * D_ * 2), ro = mk_rsrc(p.wo, D_ * D_ * 2);
      unsigned vlo[5];
#pragma unroll
      for (int j = 0; j < 5; ++j) {
        const int Lc = ((w & 3) * 5 + j) * 64 + lane;
        vlo[j] = (unsigned)((Lc / 80) * 1280 + (Lc % 80) * 16);
      }
      auto issue = [&](int sl) {
        const int s = rt + 16 * sl;
        int b0, b1;
        if (s < NKH) {
          const int u = s / 6, e = s % 6;
          if (e < 4) { b0 = 192 * u + 48 * e; b1 = b0 + 16; } else { b0 = 192 * u + 96 * (e - 4) + 32; b1 = b0 + 48; }
        } else { b0 = 32 * (s - NKH); b1 = b0 + 16; }
        const __amdgpu_buffer_rsrc_t rs = s < NKH ? rd : ro;
        const unsigned so = (unsigned)((w < 4 ? b0 : b1) * 1280), m0 = (unsigned)((sl % 3) * 40960 + w * 5120);
#pragma unroll
        for (int j = 0; j < 5; ++j) LDSDMA(m0 + j * 1024, vlo[j], rs, so);
      };
      auto retile = [&](int sl) {
        const int s = rt + 16 * sl;
        LAS char* base = L + (sl % 3) * 40960 + (4 * g + q) * 1280 + w * 160;
        const unsigned dst = s < NKS ? WT_OFF + (w * NKS + s) * 5120 + lane * 16 : 0x80000000u;
#pragma unroll
        for (int nb = 0; nb < 5; ++nb) {
          const int col = nb < 4 ? 32 * (nb >> 1) + 8 * pp + 4 * (nb & 1) : 64 + 4 * pp;
          st16(rws, dst + nb * 1024, tr_read2(base + col * 2, 16 * 1280));
        }
      };
      SYNCL();
      for (int i = 0; i < 3; ++i) __builtin_amdgcn_s_sleep(16);
#define AUX_SYNC(n, k) do { asm volatile("s_waitcnt vmcnt(" #n ")" ::: "memory"); lds_arrive(L, C_KV1); lds_wait(L, C_KV1, 8 * (k)); } while (0)
      issue(0); issue(1); issue(2);
      AUX_SYNC(10, 1); retile(0);
      AUX_SYNC(10, 2); issue(3); retile(1);
      AUX_SYNC(15, 3); issue(4); retile(2);
      AUX_SYNC(15, 4); retile(3);
      AUX_SYNC(10, 5); retile(4);
    }
  } else {
    const int mh = w & 1, n = w >> 1;
    const int rt64 = rt * 2 + mh;
    if (isGU) {
#define FRAG_VARS LANE_VARS(fresh_lane()); const int sw = ((q >> 1) & 1) | ((g & 1) << 1); \
  const unsigned xoff0 = (unsigned)((64 * mh + tok) * 128 + ((g ^ (tok >> 1)) * 16))
      f32x4 acc[4][6];
      {
        FRAG_VARS;
        unsigned aoff[6];
#pragma unroll
        for (int b = 0; b < 3; ++b) {
          const int ch = 6 * n + 2 * b + (pp >> 1);
          aoff[b] = (unsigned)((8 * g + q) * 384 + (ch >> 3) * 128 + (((ch & 7) ^ (2 * sw)) * 16) + (pp & 1) * 8);
        }
        aoff[3] = aoff[4] = aoff[5] = 0;
        k1_mainloop<0>(acc, L, aoff, xoff0);
      }
      const int lane = fresh_lane();
      const unsigned actA = ACT_OFF + ((rt64 * NKS + 6 * cs + n) * 4) * 1024 + lane * 16;
      const unsigned actB = ACT_OFF + ((rt64 * NKS + 6 * cs + 4 + (n >> 1)) * 4 + 2 * (n & 1)) * 1024 + lane * 16;
      v2u h2[4];
#pragma unroll
      for (int m = 0; m < 4; ++m) h2[m] = gelu_mul4(acc[m][4], acc[m][5]);
      const bool odd = n & 1;
      *(LAS v4u*)(L + L_WPHI + w * 1024 + lane * 16) = odd ? v4u{h2[0].x, h2[0].y, h2[1].x, h2[1].y} : v4u{h2[2].x, h2[2].y, h2[3].x, h2[3].y};
      const unsigned xw = C_X + 4 * ((w & 1) | ((w >> 2) << 1));
      lds_arrive(L, xw);
      const v2u k0 = odd ? h2[2] : h2[0], k1 = odd ? h2[3] : h2[1];
#pragma unroll
      for (int m = 0; m < 4; ++m) {
        const v2u h0 = gelu_mul4(acc[m][0], acc[m][1]), h1 = gelu_mul4(acc[m][2], acc[m][3]);
        st16(rws, actA + m * 1024, v4u{h0.x, h0.y, h1.x, h1.y});
        if (m == 2) {
          lds_wait(L, xw, 2);
          const v4u o = *(LAS v4u*)(L + L_WPHI + (w ^ 2) * 1024 + lane * 16);
          st16(rws, actB, odd ? v4u{o.x, o.y, k0.x, k0.y} : v4u{k0.x, k0.y, o.x, o.y});
          st16(rws, actB + 1024, odd ? v4u{o.z, o.w, k1.x, k1.y} : v4u{k1.x, k1.y, o.z, o.w});
        }
      }
    } else {
#define QKV_AOFF \
    FRAG_VARS; \
    unsigned aoff[6]; \
    { \
      aoff[5] = L_WPHI + g * 16; \
      const int offch = isKV ? ((cs - 11) & 1) * 4 : (n >> 1) * 4; \
      _Pragma("unroll") for (int b = 0; b < 5; ++b) { \
        const int u = 2 * offch + 20 * (n & 1) + 5 * pp + b, ch = u >> 1; \
        aoff[b] = (unsigned)((n >> 1) * 12288 + (8 * g + q) * 384 + (ch >> 3) * 128 + (((ch & 7) ^ (2 * sw)) * 16) + (u & 1) * 8); \
      } \
    }
      v4u c01[4], c23[4];
      v2u c4[4];
      if (cs == 9) {
        f32x4 acc[4][6];
        {
          QKV_AOFF
          k1_mainloop<2>(acc, L, aoff, xoff0);
        }
#pragma unroll
        for (int m = 0; m < 4; ++m) { c01[m] = cvt8(acc[m][0], acc[m][1]); c23[m] = cvt8(acc[m][2], acc[m][3]); c4[m] = cvt4(acc[m][4]); }
        if (n == 0) {
          LANE_VARS(fresh_lane());
          const float a = g == 0 ? acc[0][5][0] : g == 1 ? acc[1][5][0] : g == 2 ? acc[2][5][0] : acc[3][5][0];
          __builtin_amdgcn_raw_buffer_store_b32(f2u(1.0f / (1.0f + __expf(-a))), rws, G_OFF + (rt64 * 64 + lane) * 4, 0, 16);
        }
      } else {
        f32x4 acc[4][5];
        {
          QKV_AOFF
          k1_mainloop<1>(acc, L, aoff, xoff0);
        }
#pragma unroll
        for (int m = 0; m < 4; ++m) { c01[m] = cvt8(acc[m][0], acc[m][1]); c23[m] = cvt8(acc[m][2], acc[m][3]); c4[m] = cvt4(acc[m][4]); }
      }
      LANE_VARS(fresh_lane());
      if (!isKV) {
          const int head = 16 * (cs - 9) + 4 * n + g;
        const unsigned qp = Q_OFF + (head * 32 + rt64) * 2560;
#pragma unroll
        for (int pr = 0; pr < 2; ++pr) {
          const unsigned o = qp + pr * 1024 + tok * 16;
          st16(rws, o, v4u{c01[2 * pr].x, c01[2 * pr].y, c01[2 * pr + 1].x, c01[2 * pr + 1].y});
          st16(rws, o + 256, v4u{c01[2 * pr].z, c01[2 * pr].w, c01[2 * pr + 1].z, c01[2 * pr + 1].w});
          st16(rws, o + 512, v4u{c23[2 * pr].x, c23[2 * pr].y, c23[2 * pr + 1].x, c23[2 * pr + 1].y});
          st16(rws, o + 768, v4u{c23[2 * pr].z, c23[2 * pr].w, c23[2 * pr + 1].z, c23[2 * pr + 1].w});
        }
        const unsigned o = qp + 2048 + tok * 8;
        st8(rws, o, v2u{(c4[0].x & 0xffffu) | (c4[1].x << 16), (c4[2].x & 0xffffu) | (c4[3].x << 16)});
        st8(rws, o + 128, v2u{(c4[0].x >> 16) | (c4[1].x & 0xffff0000u), (c4[2].x >> 16) | (c4[3].x & 0xffff0000u)});
        st8(rws, o + 256, v2u{(c4[0].y & 0xffffu) | (c4[1].y << 16), (c4[2].y & 0xffffu) | (c4[3].y << 16)});
        st8(rws, o + 384, v2u{(c4[0].y >> 16) | (c4[1].y & 0xffff0000u), (c4[2].y >> 16) | (c4[3].y & 0xffff0000u)});
      } else {
        const int hl = 4 * (n & 1) + g;
        LAS char* xch = L + L_DUMMY + (w & 3) * 5120;
        if (n < 2) {
#pragma unroll
          for (int mm = 0; mm < 2; ++mm) {
            *(LAS v4u*)(xch + mm * 2560 + lane * 16) = c01[2 + mm];
            *(LAS v4u*)(xch + mm * 2560 + 1024 + lane * 16) = c23[2 + mm];
            *(LAS v2u*)(xch + mm * 2560 + 2048 + lane * 8) = c4[2 + mm];
          }
        }
        lds_arrive(L, C_KV1);
        v4u wf[5][3];
        {
          LAS char* wb = L + (pp == g ? L_WPHI + (unsigned)(hl * 800 + q * 40) : L_ZERO);
#pragma unroll
          for (int bo = 0; bo < 5; ++bo)
#pragma unroll
            for (int s3 = 0; s3 < 3; ++s3) {
              v2u lo = tr_read(wb + s3 * 320 + bo * 8), hi = v2u{0u, 0u};
              if (s3 < 2) hi = tr_read(wb + s3 * 320 + 160 + bo * 8);
              wf[bo][s3] = v4u{lo.x, lo.y, hi.x, hi.y};
            }
        }
        lds_wait(L, C_KV1, 8);
        if (n >= 2) {
#pragma unroll
          for (int m = 0; m < 4; ++m) {
            LAS char* dst = L + L_V + (hl * 128 + 64 * mh + 16 * m + tok) * 48;
            *(LAS v4u*)(dst) = c01[m];
            *(LAS v4u*)(dst + 16) = c23[m];
            *(LAS v4u*)(dst + 32) = v4u{c4[m].x, c4[m].y, 0x00003f80u, 0u};
          }
#pragma unroll
          for (int mm = 0; mm < 2; ++mm) {
            c01[mm] = *(LAS v4u*)(xch + mm * 2560 + lane * 16);
            c23[mm] = *(LAS v4u*)(xch + mm * 2560 + 1024 + lane * 16);
            c4[mm] = *(LAS v2u*)(xch + mm * 2560 + 2048 + lane * 8);
          }
        }
        {
          LAS char* dst0 = L + L_PK + (hl * 128 + 64 * mh + (n >= 2 ? 32 : 0) + tok) * 80;
          f32x4 pk[2][5];
#pragma unroll
          for (int m = 0; m < 2; ++m) {
            const v4u kf[3] = {c01[m], c23[m], v4u{c4[m].x, c4[m].y, 0u, 0u}};
#pragma unroll
            for (int bo = 0; bo < 5; ++bo) {
              pk[m][bo] = f32x4{0.f, 0.f, 0.f, 0.f};
#pragma unroll
              for (int s3 = 0; s3 < 3; ++s3) pk[m][bo] = mfma(wf[bo][s3], kf[s3], pk[m][bo]);
            }
          }
#pragma unroll
          for (int m = 0; m < 2; ++m) {
            float mx = pk[m][0][0];
#pragma unroll
            for (int bo = 0; bo < 5; ++bo)
#pragma unroll
              for (int r = 0; r < 4; ++r) mx = fmaxf(mx, pk[m][bo][r]);
            const float l2e = 1.4426950408889634f;
            const float mxs = -mx * l2e;
            f32x2 ep[10], en[10];
#pragma unroll
            for (int i = 0; i < 10; ++i) ep[i] = exp2v(f32x2{pk[m][i >> 1][2 * (i & 1)], pk[m][i >> 1][2 * (i & 1) + 1]} * l2e + mxs);
            f32x2 sj[5], rj[5];
#pragma unroll
            for (int j = 0; j < 5; ++j) {
              sj[j] = ep[j] + ep[5 + j];
              const f32x2 t = ep[j] * ep[5 + j];
              rj[j] = f32x2{__builtin_amdgcn_rcpf(t[0]), __builtin_amdgcn_rcpf(t[1])};
            }
            const f32x2 sp = ((sj[0] + sj[1]) + (sj[2] + sj[3])) + sj[4];
            const f32x2 sn = sj[4] * rj[4] + (sj[3] * rj[3] + (sj[2] * rj[2] + (sj[1] * rj[1] + sj[0] * rj[0])));
            const float snt = sn[0] + sn[1], rp = __builtin_amdgcn_rcpf(sp[0] + sp[1]);
            if (__builtin_expect(__any(!(snt < 0x1p60f)), 0)) {
              float mn = pk[m][0][0];
#pragma unroll
              for (int bo = 0; bo < 5; ++bo)
#pragma unroll
                for (int r = 0; r < 4; ++r) mn = fminf(mn, pk[m][bo][r]);
              const float mns = mn * l2e;
#pragma unroll
              for (int i = 0; i < 10; ++i) en[i] = exp2v(f32x2{pk[m][i >> 1][2 * (i & 1)], pk[m][i >> 1][2 * (i & 1) + 1]} * -l2e + mns);
              const f32x2 s2 = (((en[0] + en[1]) + (en[2] + en[3])) + ((en[4] + en[5]) + (en[6] + en[7]))) + (en[8] + en[9]);
              const float rn = __builtin_amdgcn_rcpf(s2[0] + s2[1]);
#pragma unroll
              for (int i = 0; i < 10; ++i) en[i] *= rn;
            } else {
              const float rn = __builtin_amdgcn_rcpf(snt);
#pragma unroll
              for (int j = 0; j < 5; ++j) { const f32x2 t = rj[j] * rn; en[j] = t * ep[5 + j]; en[5 + j] = t * ep[j]; }
            }
            unsigned o[20];
#pragma unroll
            for (int i = 0; i < 10; ++i) { o[i] = cvtpk(ep[i] * rp); o[10 + i] = cvtpk(en[i]); }
            LAS char* dst = dst0 + m * 16 * 80;
#pragma unroll
            for (int i = 0; i < 5; ++i) *(LAS v4u*)(dst + i * 16) = v4u{o[4 * i], o[4 * i + 1], o[4 * i + 2], o[4 * i + 3]};
          }
        }
        lds_arrive(L, C_KV2);
        lds_wait(L, C_KV2, 8);
        if (w == 7) hlook = __builtin_amdgcn_raw_buffer_load_b64(rws, lane < 10 ? lane * FS : 0x80000000u, FL_RT + ((xcd >> 1) * 4 + ((idx & 7) >> 1)) * 16 * FS, 16);
        SB0();
        {
          f32x4 kv[3][2];
#pragma unroll
          for (int fb = 0; fb < 3; ++fb) { kv[fb][0] = f32x4{0.f, 0.f, 0.f, 0.f}; kv[fb][1] = f32x4{0.f, 0.f, 0.f, 0.f}; }
          LAS char* pa = L + L_PK + (w * 128 + 8 * g + q) * 80 + pp * 8;
          LAS char* pb = L + L_V + (w * 128 + 8 * g + q) * 48 + pp * 8;
#pragma unroll
          for (int ks = 0; ks < 4; ++ks) {
            v4u af[3], bf[2];
#pragma unroll
            for (int fb = 0; fb < 3; ++fb) af[fb] = tr_read2(pa + ks * 32 * 80 + fb * 32, 4 * 80);
#pragma unroll
            for (int vb = 0; vb < 2; ++vb) bf[vb] = tr_read2(pb + ks * 32 * 48 + vb * 32, 4 * 48);
#pragma unroll
            for (int fb = 0; fb < 3; ++fb)
#pragma unroll
              for (int vb = 0; vb < 2; ++vb) kv[fb][vb] = mfma(af[fb], bf[vb], kv[fb][vb]);
          }
          const int head = 8 * (cs - 11) + w;
          const unsigned rec = KVP_OFF + (head * 16 + rt) * KVP_REC;
          const unsigned o4 = rec + tok * 64 + g * 16, o2 = g < 2 ? rec + tok * 16 + g * 8 : 0x80000000u;
          const unsigned t5 = tok < 5 ? 0u : 0x80000000u;
          st16(rws, o4, cvt8(kv[0][0], kv[1][0]));
          st8(rws, o2 + 1024, cvt4(kv[2][0]));
          st16(rws, (o4 | t5) + 1280, cvt8(kv[0][1], kv[1][1]));
          st8(rws, (o2 | t5) + 1600, cvt4(kv[2][1]));
            }
      }
    }
  }
  }

  if (isAUX && w >= 8) lds_wait(L, C_DRAIN, 8);
  v2u tag;
  {
    const v2u e = *(volatile LAS v2u*)(L + L_CTL + C_TAG);
    unsigned long long t = (((unsigned long long)rfl(e.y) << 32) | rfl(e.x)) * 0x9E3779B97F4A7C15ull + 0xD1B54A32D192ED03ull;
    t ^= t >> 29;
    tag = v2u{(unsigned)t | 1u, (unsigned)(t >> 32)};
  }
  const int rt64 = (xcd >> 1) * 8 + (idx & 7), ct = (xcd & 1) * 4 + (idx >> 3);
  const __amdgpu_buffer_rsrc_t rw = mk_rsrc(ws + WT_OFF + ct * (NKS * 5120), NKH * 5120);
  const f32x4 z4 = {0.f, 0.f, 0.f, 0.f};

  if (w < 8) {
    if (w == 7) *(volatile LAS unsigned*)(L + L_CTL + C_HOK) = (unsigned)__builtin_amdgcn_ballot_w64(hlook.x == tag.x && hlook.y == tag.y) & 0x3ffu;
    lds_arrive(L, C_PRE);
    const int lane = fresh_lane();
    unsigned off = 0x80000000u;
    if (lane == 0) off = FL_ALL + bid * 8;
    else if (isGU) { if (lane == 1) off = FL_RT + (rt * 16 + cs) * FS; }
    else if (isAUX) { if (lane <= 8) off = FL_AUX + ((lane - 1) * 16 + rt) * FS; }
    else if (isKV) { if (lane <= 2) off = FL_KV + (((cs - 11) * 2 + lane - 1) * 16 + rt) * FS; }
    else if (lane <= 8) off = FL_Q + ((lane - 1) * 32 + rt * 2 + cs - 9) * FS;
    else if (lane == 9) off = FL_RT + (rt * 16 + cs) * FS;
    v2u tv = tag;
    unsigned old = lane == 0 ? 1u : 0u, la = L_CTL + C_DRAIN;
    asm volatile("s_waitcnt vmcnt(0) lgkmcnt(0)\n\ts_mov_b64 exec, 1\n\tds_add_rtn_u32 %0, %1, %0\n\ts_mov_b64 exec, -1\n\ts_waitcnt lgkmcnt(0)"
                 : "+v"(old), "+v"(la), "+v"(off), "+v"(tv) :: "memory");
    if (rfl(old) == 7u) st8(rws, off, tv);
  }

  if (w >= 8) {
    LANE_VARS(fresh_lane());
    const unsigned vo = lane * 16;
    const int lw = w - 8;
    __builtin_amdgcn_s_setprio(1);
    if (lw == 0) {
      poll(rws, FL_AUX + (xcd * 16 + tok) * FS, tag, 16);
      lds_arrive(L, C_AUXOK);
    }
    v4u wo_[15];
    {
      if (lw != 0) lds_wait(L, C_AUXOK, 1);
      const __amdgpu_buffer_rsrc_t rwo = mk_rsrc(ws + WT_OFF + (ct * NKS + NKH) * 5120, (NKS - NKH) * 5120);
#pragma unroll
      for (int i = 0; i < 15; ++i) wo_[i] = __builtin_amdgcn_raw_buffer_load_b128(rwo, vo + (i & 3) * 1024, (3 * lw) * 5120 + (i >> 2) * 4096, 16);
      SB0();
    }
    const int head = xcd * 4 + (idx & 3), rg = idx >> 2;
    v4u wa0, wa1;
    {
      const u16* wp = p.wphi + head * 400;
      unsigned e0[5], e1[5];
#pragma unroll
      for (int j = 0; j < 5; ++j) {
        const int d = j < 4 ? 4 * g + j : 16 + g;
        e0[j] = wp[d * 20 + tok];
        e1[j] = wp[d * 20 + 16 + (tok >> 2)];
      }
      wa0 = v4u{e0[0] | e0[1] << 16, e0[2] | e0[3] << 16, e0[4], 0u};
      wa1 = v4u{e1[0] | e1[1] << 16, e1[2] | e1[3] << 16, e1[4], 0u};
    }
    if (lw == 1) { poll(rws, FL_Q + (xcd * 32 + 4 * rg + (head >> 4) + 2 * (lane & 1)) * FS, tag, 2); lds_arrive(L, C_T1); }
    else lds_wait(L, C_T1, 1);
    v4u qf[4];
    {
      const unsigned qs = Q_OFF + (head * 32 + rg * 4 + lw) * 2560;
      const v4u q0 = __builtin_amdgcn_raw_buffer_load_b128(rws, vo, qs, 16), q1 = __builtin_amdgcn_raw_buffer_load_b128(rws, vo + 1024, qs, 16);
      const v2u q4 = __builtin_amdgcn_raw_buffer_load_b64(rws, lane * 8 + 2048, qs, 16);
      qf[0] = v4u{q0.x, q0.y, q4.x & 0xffffu, 0u};
      qf[1] = v4u{q0.z, q0.w, q4.x >> 16, 0u};
      qf[2] = v4u{q1.x, q1.y, q4.y & 0xffffu, 0u};
      qf[3] = v4u{q1.z, q1.w, q4.y >> 16, 0u};
    }
    v4u fp[4], fn[4];
    const unsigned fko = lane < 16 ? FL_KV + (xcd * 16 + lane) * FS : FL_Q + (xcd * 32 + 4 * rg + 2 * (lane & 1)) * FS;
    v2u fkv = v2u{0u, 0u};
#pragma unroll
    for (int m = 0; m < 4; ++m) {
      if (m == 2) {
        SB0();
        fkv = __builtin_amdgcn_raw_buffer_load_b64(rws, lane >= 18 ? 0x80000000u : fko, 0, 16);
        SB0();
      }
      const f32x4 p0 = mfma(wa0, qf[m], z4);
      const float p4 = mfma(wa1, qf[m], z4)[0];
      const float mx = __builtin_fmaxf(__builtin_fmaxf(__builtin_fmaxf(p0[0], p0[1]), p0[2]), __builtin_fmaxf(p0[3], p4));
      const float mn = __builtin_fminf(__builtin_fminf(__builtin_fminf(p0[0], p0[1]), p0[2]), __builtin_fminf(p0[3], p4));
      const float l2e = 1.4426950408889634f;
      const float mxs = red4_max(mx) * -l2e, mns = red4_max(-mn) * -l2e;
      const f32x2 x0 = f32x2{p0[0], p0[1]}, x1 = f32x2{p0[2], p0[3]};
      const f32x2 ep0 = exp2v(x0 * l2e + mxs), ep1 = exp2v(x1 * l2e + mxs), en0 = exp2v(x0 * -l2e + mns), en1 = exp2v(x1 * -l2e + mns);
      const f32x2 e4 = exp2v(f32x2{p4, p4} * f32x2{l2e, -l2e} + f32x2{mxs, mns});
      const f32x2 sp = ep0 + ep1, sn = en0 + en1;
      const float rp = __builtin_amdgcn_rcpf(red4_sum((sp[0] + sp[1]) + e4[0])), rn = __builtin_amdgcn_rcpf(red4_sum((sn[0] + sn[1]) + e4[1]));
      fp[m] = v4u{cvtpk(ep0 * rp), cvtpk(ep1 * rp), cvtpk(en0 * rn), cvtpk(en1 * rn)};
      fn[m] = v4u{cvtpk(e4 * f32x2{rp, rn}), 0u, 0u, 0u};
      asm volatile("" : "+v"(fp[m]), "+v"(fn[m]));
    }
    const int rto = rg * 4 + lw;
    const int Q0 = 5 * head + g, Q1 = 5 * head + 4;
    unsigned o0 = (unsigned)(ACT_OFF + (rto * NKS + NKH + (Q0 >> 3)) * 4096 + (Q0 & 7) * 256 + tok * 16);
    unsigned o1 = g == 0 ? (unsigned)(ACT_OFF + (rto * NKS + NKH + (Q1 >> 3)) * 4096 + (Q1 & 7) * 256 + tok * 16) : 0x80000000u;
    asm volatile("" : "+v"(o0), "+v"(o1));
    unsigned ko, kw, kr0, kr1;
    {
      const int c = lw * 32 + (lane & 31);
      ko = c < 105 ? (unsigned)(c * 16 + (lane >> 5) * 8 * KVP_REC) : 0x80000000u;
      const int t = 2 * c + (lane >> 5);
      int d, f0;
      if (t < 128) { d = t >> 3; f0 = 16 * (t & 1) + 4 * ((t >> 1) & 3); }
      else if (t < 160) { d = (t - 128) >> 1; f0 = 32 + 4 * (t & 1); }
      else if (t < 200) { const int u = t - 160; d = 16 + (u >> 3); f0 = 16 * (u & 1) + 4 * ((u >> 1) & 3); }
      else { d = 16 + ((t - 200) >> 1); f0 = 32 + 4 * (t & 1); }
      kw = t < 210 ? L_KVS + d * 80 + f0 * 2 : L_KVS + 1696;
      kr0 = L_KVS + tok * 80; kr1 = L_KVS + (tok < 4 ? 16 + tok : 20) * 80;
      asm volatile("" : "+v"(ko), "+v"(kw), "+v"(kr0), "+v"(kr1));
    }
    SB0();
    __builtin_amdgcn_s_setprio(3);
    if (__any(lane < 18 && (fkv.x != tag.x || fkv.y != tag.y))) {
      if (lw == 0) {
        poll(rws, fko, tag, 18);
        lds_arrive(L, C_KVOK);
      } else lds_wait(L, C_KVOK, 1);
    } else if (lw == 0) lds_arrive(L, C_KVOK);
    __builtin_amdgcn_fence(__ATOMIC_ACQUIRE, "wavefront");
    const float gl = u2f(__builtin_amdgcn_raw_buffer_load_b32(rws, lane * 4, G_OFF + (rg * 256 + lw * 64) * 4, 16));
    float gm[4];
    {
      v4u v[8];
#pragma unroll
      for (int r = 0; r < 8; ++r) v[r] = __builtin_amdgcn_raw_buffer_load_b128(rws, ko, KVP_OFF + (head * 16 + r) * KVP_REC, 16);
      SB0();
#pragma unroll
      for (int m = 0; m < 4; ++m) gm[m] = u2f(__builtin_amdgcn_ds_bpermute((m * 16 + tok) * 4, f2u(gl)));
      SB0();
      f32x4 s0, s1;
#pragma unroll
      for (int r = 0; r < 8; ++r) {
        const f32x4 a0 = f32x4{u2f(v[r].x << 16), u2f(v[r].x & 0xffff0000u), u2f(v[r].y << 16), u2f(v[r].y & 0xffff0000u)};
        const f32x4 a1 = f32x4{u2f(v[r].z << 16), u2f(v[r].z & 0xffff0000u), u2f(v[r].w << 16), u2f(v[r].w & 0xffff0000u)};
        s0 = r ? s0 + a0 : a0; s1 = r ? s1 + a1 : a1;
      }
      f32x4 s;
#pragma unroll
      for (int e = 0; e < 4; ++e) {
        auto x = __builtin_amdgcn_permlane32_swap(f2u(s0[e]), f2u(s1[e]), false, false);
        const unsigned x0 = x[0], x1 = x[1];
        s[e] = u2f(x0) + u2f(x1);
      }
      *(LAS v2u*)(L + kw) = cvt4(s);
    }
    lds_arrive(L, C_OSYNC);
    lds_wait(L, C_OSYNC, 4);
    {
      v4u ka[2][2];
#pragma unroll
      for (int b = 0; b < 2; ++b) {
        LAS char* src = L + (b == 0 ? kr0 : kr1);
        const v2u lo = *(LAS v2u*)(src + g * 8), hi = *(LAS v2u*)(src + 40 + g * 8);
        const unsigned u0 = *(LAS u16*)(src + 32 + g * 2), u1 = *(LAS u16*)(src + 72 + g * 2);
        ka[b][0] = v4u{lo.x, lo.y, hi.x, hi.y};
        ka[b][1] = v4u{u0 | u1 << 16, 0u, 0u, 0u};
      }
      SB0();
      v2u a[4], b[4];
      f32x4 n0[4], n1[4];
#pragma unroll
      for (int m = 0; m < 4; ++m) n1[m] = mfma(ka[1][0], fp[m], z4);
#pragma unroll
      for (int m = 0; m < 4; ++m) n1[m] = mfma(ka[1][1], fn[m], n1[m]);
#pragma unroll
      for (int m = 0; m < 4; ++m) n0[m] = mfma(ka[0][0], fp[m], z4);
#pragma unroll
      for (int m = 0; m < 4; ++m) n0[m] = mfma(ka[0][1], fn[m], n0[m]);
      SB0();
#pragma unroll
      for (int m = 0; m < 4; ++m) {
        auto r = __builtin_amdgcn_permlane16_swap(f2u(n1[m][0]), f2u(n1[m][0]), false, false);
        const unsigned r1 = r[1];
        const float sc = gm[m] * __builtin_amdgcn_rcpf(u2f(r1) + 1e-6f);
        a[m] = cvt4(n0[m] * sc); b[m] = cvt4(n1[m] * sc);
        if (m & 1) {
          st16(rws, o0 + (m >> 1) * 2048, v4u{a[m - 1].x, a[m - 1].y, a[m].x, a[m].y});
          st16(rws, o1 + (m >> 1) * 2048, v4u{b[m - 1].x, b[m - 1].y, b[m].x, b[m].y});
        }
      }
    }
    SB0();
    {
      lds_wait(L, C_PRE, 8);
#pragma unroll
      for (int i = 0; i < 15; ++i) *(LAS v4u*)(L + lw * 20480 + i * 1024 + vo) = wo_[i];
    }
    lds_arrive(L, C_CONS);
    asm volatile("s_waitcnt vmcnt(0)" ::: "memory");
    if (lane == 0) st8(rws, FL_O + ((rg * 4 + lw) * 32 + head) * FS, tag);
    if (lw == 1) {
      unsigned fo = lane < 32 ? FL_O + (rt64 * 32 + lane) * FS : 0x80000000u;
#pragma unroll 1
      for (unsigned spins = 0; spins < (1u << 18); ++spins) {
        const v2u v = __builtin_amdgcn_raw_buffer_load_b64(rws, fo, 0, 16);
        if (v.x == tag.x && v.y == tag.y) fo = 0x80000000u;
        const unsigned ready = (unsigned)__builtin_amdgcn_ballot_w64(fo == 0x80000000u);
        *(volatile LAS unsigned*)(L + L_CTL + C_OOK) = ready;
        if (ready == 0xffffffffu) break;
        __builtin_amdgcn_s_sleep(POLL_SLEEP);
      }
    }
    if (bid == 0 && lw == 0) {
#pragma unroll 1
      for (unsigned spins = 0; spins < (1u << 18); ++spins) {
        bool ok = true;
#pragma unroll
        for (int i = 0; i < 2; ++i) {
          const v4u v = __builtin_amdgcn_raw_buffer_load_b128(rws, vo + i * 1024, FL_ALL, 16);
          ok &= v.x == tag.x && v.y == tag.y && v.z == tag.x && v.w == tag.y;
        }
        if (__all(ok)) break;
        __builtin_amdgcn_s_sleep(POLL_SLEEP);
      }
      if (lane == 0) st8(rws, FL_EPOCH, tag);
    }
    return;
  }

  __builtin_amdgcn_s_setprio(0);

  const __amdgpu_buffer_rsrc_t ra = mk_rsrc(ws + ACT_OFF + rt64 * (NKS * 4096), NKH * 4096);
  const __amdgpu_buffer_rsrc_t rao = mk_rsrc(ws + ACT_OFF + (rt64 * NKS + NKH) * 4096, (NKS - NKH) * 4096);
  const __amdgpu_buffer_rsrc_t rwo = mk_rsrc(ws + WT_OFF + (ct * NKS + NKH) * 5120, (NKS - NKH) * 5120);
  const int h0 = 7 * w, o0 = w < 4 ? 3 * w : 4 + 2 * w;
  const unsigned vo = fresh_lane() * 16;
  Stage st[2];
#define P2_WO() do { lds_wait(L, C_AUXOK, 1); lds_wait(L, C_PRE, 8); \
    _Pragma("unroll") for (int i = 0; i < 4; ++i) { const unsigned m_ = w * 20480 + i * 4096, so_ = (unsigned)o0 * 5120 + i * 4096; \
      DMAS(m_, vo, rwo, so_, 0); DMAS(m_, vo, rwo, so_, 1024); if (i == 2 && w >= 4) break; DMAS(m_, vo, rwo, so_, 2048); if (i < 3) DMAS(m_, vo, rwo, so_, 3072); } } while (0)
  if (w == 7) {
    __builtin_amdgcn_s_setprio(3);
    unsigned fo = vo < 160 ? (vo >> 4) * FS : 0x80000000u;
    if (hlook.x == tag.x && hlook.y == tag.y) fo = 0x80000000u;
#pragma unroll 1
    for (unsigned spins = 0; spins < (1u << 18) && __any(fo != 0x80000000u); ++spins) {
      const v2u v = __builtin_amdgcn_raw_buffer_load_b64(rws, fo, FL_RT + (rt64 >> 1) * 16 * FS, 16);
      if (v.x == tag.x && v.y == tag.y) fo = 0x80000000u;
      const unsigned ready = (unsigned)__builtin_amdgcn_ballot_w64(fo == 0x80000000u) & 0x3ffu;
      *(volatile LAS unsigned*)(L + L_CTL + C_HOK) = ready;
      if (ready == 0x3ffu) break;
      __builtin_amdgcn_s_sleep(POLL_SLEEP);
    }
    __builtin_amdgcn_s_setprio(0);
  }
  f32x4 acc[5][4];
  lds_wait(L, C_AUXOK, 1);
  const bool wo_early = w != 7;
  if (wo_early && w >= 4) P2_WO();
  p2_load_a(st[0], rw, vo, h0);
  SB0();
  p2_load_a(st[1], rw, vo, h0 + 1);
  SB0();
  {
    const unsigned need = (3u << (h0 / 6)) | 0x200u;
    lds_wait_bits(L, C_HOK, need);
  }
  p2_load_b(st[0], ra, vo, h0);
  SB0();
  p2_load_b(st[1], ra, vo, h0 + 1);
  float gl = 0.f;
  SB0();
#pragma unroll
  for (int i = 0; i < 7; ++i) {
    Stage& cur = st[i & 1];
#pragma unroll
    for (int nb = 0; nb < 5; ++nb)
#pragma unroll
      for (int m = 0; m < 4; ++m) {
        if (i == 0) mfma_init(acc[nb][m], cur.a[nb], cur.b[m]);
        else mfma_ip2(acc[nb][m], cur.a[nb], cur.b[m]);
      }
    SB0();
    const int nx = i + 2;
    if (nx == 6) gl = u2f(__builtin_amdgcn_raw_buffer_load_b32(rws, vo >> 2, G_OFF + rt64 * 256, 16));
    if (nx < 7) { p2_load_b(cur, ra, vo, h0 + nx); p2_load_a(cur, rw, vo, h0 + nx); }
    SB0();
  }
  asm volatile("s_nop 15" ::: "memory");
  SB0();
  if (!wo_early && w >= 4) P2_WO();
  SB0();
  {
    const int tk4 = (fresh_lane() & 15) * 4;
#pragma unroll
    for (int m = 0; m < 4; ++m) {
      const float gh = 1.0f - u2f(__builtin_amdgcn_ds_bpermute(m * 64 + tk4, f2u(gl)));
#pragma unroll
      for (int nb = 0; nb < 5; ++nb) { acc[nb][m] *= gh; asm volatile("" : "+v"(acc[nb][m])); }
    }
  }
  SB0();
  {
    const int hl = 8 * o0 / 5, hh = (8 * o0 + (w < 4 ? 23 : 15)) / 5;
    const unsigned need = (2u << hh) - (1u << hl);
    lds_wait_bits(L, C_OOK, need);
  }
  {
    v4u b[3][4];
#pragma unroll
    for (int i = 0; i < 3; ++i) {
#pragma unroll
      for (int m = 0; m < 4; ++m) b[i][m] = __builtin_amdgcn_raw_buffer_load_b128(rao, vo + m * 1024, (i == 2 && w >= 4 ? NKS : o0 + i) * 4096, 16);
      SB0();
    }
    asm volatile("s_waitcnt vmcnt(12)" ::: "memory");
    if (w < 4) lds_wait(L, C_CONS, 4);
    SB0();
    LAS char* ar = L + w * 20480 + vo;
#pragma unroll
    for (int i = 0; i < 3; ++i) {
      if (i == 2 && w >= 4) break;
      v4u a[5];
#pragma unroll
      for (int nb = 0; nb < 5; ++nb) a[nb] = *(LAS v4u*)(ar + (i * 5 + nb) * 1024);
#pragma unroll
      for (int pr = 0; pr < 2; ++pr) {
        const unsigned tx = b[i][2 * pr].z, ty = b[i][2 * pr].w;
        b[i][2 * pr].z = b[i][2 * pr + 1].x; b[i][2 * pr].w = b[i][2 * pr + 1].y;
        b[i][2 * pr + 1].x = tx; b[i][2 * pr + 1].y = ty;
      }
#pragma unroll
      for (int nb = 0; nb < 5; ++nb)
#pragma unroll
        for (int m = 0; m < 4; ++m) mfma_ip2(acc[nb][m], a[nb], b[i][m]);
      SB0();
    }
  }
  asm volatile("s_nop 15" ::: "memory");
  SB0();
#pragma unroll
  for (int m = 0; m < 3; ++m)
#pragma unroll
    for (int nb = 0; nb < 5; ++nb) *(LAS f32x4*)(L + ((w * 4 + m) * 5 + nb) * 1024 + vo) = acc[nb][m];
  if (w < 7) {
#pragma unroll
    for (int nb = 0; nb < 5; ++nb) *(LAS f32x4*)(L + ((w * 4 + 3) * 5 + nb) * 1024 + vo) = acc[nb][3];
  }
  SYNCL();
  {
    LANE_VARS(fresh_lane());
    const __amdgpu_buffer_rsrc_t ro_ = mk_rsrc(p.out, S_ * D_ * 2);
    const unsigned op = (unsigned)(((rt64 * 64 + tok) * D_ + ct * 80) * 2);
    if (w < 6) {
      const int m = w % 3, hf = w / 3;
      LAS char* src = L + (m * 5 + hf * 2) * 1024 + vo;
      f32x4 r0 = *(LAS f32x4*)(src), r1 = *(LAS f32x4*)(src + 1024);
#pragma unroll
      for (int sw = 1; sw < 8; ++sw) { r0 += *(LAS f32x4*)(src + sw * 20480); r1 += *(LAS f32x4*)(src + sw * 20480 + 1024); }
      __builtin_amdgcn_raw_buffer_store_b128(cvt8(r0, r1), ro_, op + m * 16 * D_ * 2 + hf * 64 + 16 * g, 0, 0);
    } else if (w == 6) {
#pragma unroll
      for (int m = 0; m < 3; ++m) {
        LAS char* src = L + (m * 5 + 4) * 1024 + vo;
        f32x4 r = *(LAS f32x4*)(src);
#pragma unroll
        for (int sw = 1; sw < 8; ++sw) r += *(LAS f32x4*)(src + sw * 20480);
        __builtin_amdgcn_raw_buffer_store_b64(cvt4(r), ro_, op + m * 16 * D_ * 2 + 128 + 8 * g, 0, 0);
      }
    } else {
#pragma unroll
      for (int sw = 0; sw < 7; ++sw)
#pragma unroll
        for (int nb = 0; nb < 5; ++nb) acc[nb][3] += *(LAS f32x4*)(L + ((sw * 4 + 3) * 5 + nb) * 1024 + vo);
      __builtin_amdgcn_raw_buffer_store_b128(cvt8(acc[0][3], acc[1][3]), ro_, op + 3 * 16 * D_ * 2 + 16 * g, 0, 0);
      __builtin_amdgcn_raw_buffer_store_b128(cvt8(acc[2][3], acc[3][3]), ro_, op + 3 * 16 * D_ * 2 + 64 + 16 * g, 0, 0);
      __builtin_amdgcn_raw_buffer_store_b64(cvt4(acc[4][3]), ro_, op + 3 * 16 * D_ * 2 + 128 + 8 * g, 0, 0);
    }
  }
}

extern "C" void kernel_launch(void* const* d_in, const int* in_sizes, int n_in, void* d_out, int out_size, void* d_ws,
                              size_t ws_size, hipStream_t stream) {
  Args a;
  a.x = (const u16*)d_in[0]; a.wqkv = (const u16*)d_in[1]; a.wphi = (const u16*)d_in[2]; a.wo = (const u16*)d_in[3];
  a.wg = (const u16*)d_in[5]; a.wu = (const u16*)d_in[6]; a.wd = (const u16*)d_in[7]; a.gw = (const u16*)d_in[8];
  a.ws = (char*)d_ws;
  a.out = (u16*)d_out;
  (void)hipFuncSetAttribute((const void*)fused, hipFuncAttributeMaxDynamicSharedMemorySize, K1_LDS);
  fused<<<256, 768, K1_LDS, stream>>>(a);
}
```
